```python
import torch

def _softmax(x, axis=-1):
    return torch.softmax(x, dim=axis)
def _gelu(x):
    return torch.nn.functional.gelu(x, approximate="tanh")
def _silu(x):
    return torch.nn.functional.silu(x)
def _sigmoid(x):
    return torch.sigmoid(x)
def _layernorm(x, axis=-1, eps=1e-5):
    m = x.mean(dim=axis, keepdim=True)
    v = x.var(dim=axis, keepdim=True, unbiased=False)
    return (x - m) / torch.sqrt(v + eps)
def _scatter_add(tgt, idx, src, axis=0):
    return tgt.clone().index_add_(axis, idx, src)
def _segment_sum(src, seg, n):
    out = torch.zeros((n,) + src.shape[1:], dtype=src.dtype)
    return out.index_add_(0, seg, src)
def _pad(x, pw):
    flat = tuple(v for pair in reversed(pw) for v in pair)
    return torch.nn.functional.pad(x, flat)

kernel_name = 'recurrent_scan__gqa_block_cached__dense_activation_silu__decode'

def setup_inputs():

    _w = (lambda a: (lambda t: t.to(torch.bfloat16) if t.is_floating_point() else t)(torch.as_tensor(a)))
    def _A_setup():
        B, Sq, D = 1, 1, 512
        Hq, Hkv, Sk, Dh = 32, 32, 8192, 128
        assert Hq % Hkv == 0, (Hq, Hkv)
        return {
            "x": _w((lambda *s: torch.randn(*s, dtype=torch.bfloat16) * 0.05)(B, Sq, D)),
            "k_cache": _w((lambda *s: torch.randn(*s, dtype=torch.bfloat16) * 3.2)(B, Hkv, Sk, Dh)),
            "v_cache": _w((lambda *s: torch.randn(*s, dtype=torch.bfloat16) * 3.2)(B, Hkv, Sk, Dh)),
            "wq": _w((lambda *s: torch.randn(*s, dtype=torch.bfloat16) * 3.2)(D, Hq * Dh) * (D ** -0.5)),
            "wo": _w((lambda *s: torch.randn(*s, dtype=torch.bfloat16) * 3.2)(Hq * Dh, D) * ((Hq * Dh) ** -0.5)),
        }
    _A_in = _A_setup()

    def _B_setup():
        B, S, D, F = 1, 1, 512, 1376
        return {
            "x": _w((lambda *s: torch.randn(*s, dtype=torch.bfloat16) * 0.05)(B, S, D)),
            "w1": _w((lambda *s: torch.randn(*s, dtype=torch.bfloat16) * 3.2)(D, F) * (D ** -0.5)),
            "w2": _w((lambda *s: torch.randn(*s, dtype=torch.bfloat16) * 0.4)(F, D) * (F ** -0.5)),
        }
    _B_in = _B_setup()

    return {
        "A_x": _A_in["x"],
        "A_k_cache": _A_in["k_cache"],
        "A_v_cache": _A_in["v_cache"],
        "A_wq": _A_in["wq"],
        "A_wo": _A_in["wo"],
        "B_x": _B_in["x"],
        "B_w1": _B_in["w1"],
        "B_w2": _B_in["w2"],
    }

def reference(A_x, A_k_cache, A_v_cache, A_wq, A_wo, B_x, B_w1, B_w2):
    def _A_ref(x, k_cache, v_cache, wq, wo):
        B, Sq, D = x.shape
        Hq, Dh = 32, 128
        Hkv = k_cache.shape[1]
        rep = Hq // Hkv
        q = (lambda x, *d: x.permute(*d))((x @ wq).reshape(B, Sq, Hq, Dh), 0, 2, 1, 3)
        k = (lambda x, n, axis: torch.repeat_interleave(x, n, dim=axis))(k_cache, rep, axis=1)
        v = (lambda x, n, axis: torch.repeat_interleave(x, n, dim=axis))(v_cache, rep, axis=1)
        scores = torch.einsum("bhqd,bhkd->bhqk", q, k) * (Dh ** -0.5)
        p = _softmax(scores, axis=-1)
        o = torch.einsum("bhqk,bhkd->bhqd", p, v)
        return (lambda x, *d: x.permute(*d))(o, 0, 2, 1, 3).reshape(B, Sq, Hq * Dh) @ wo

    def _B_ref(x, w1, w2):
        h = torch.einsum("bsd,df->bsf", x, w1)
        h = _silu(h)
        out = torch.einsum("bsf,fd->bsd", h, w2)
        return out

    x = A_x
    xn = _layernorm(x, axis=-1)
    s = _A_ref(xn, A_k_cache, A_v_cache, A_wq, A_wo)
    g = _silu(_B_ref(xn, B_w1, B_w2))
    return x + s * g
```

```cpp
#include <hip/hip_runtime.h>
#include <hip/hip_bf16.h>
#include <stdint.h>

#define GAS __attribute__((address_space(1)))
#define LAS __attribute__((address_space(3)))
typedef unsigned v4u __attribute__((ext_vector_type(4)));
typedef unsigned v2u __attribute__((ext_vector_type(2)));
typedef float v4f __attribute__((ext_vector_type(4)));
typedef float v2f __attribute__((ext_vector_type(2)));
typedef __bf16 bf16x2 __attribute__((ext_vector_type(2)));

#define KV_NT 1
#define MIMIC 1
#define NPRE 16

namespace {

constexpr int D = 512, NH = 32, SK = 8192, DH = 128, FF = 1376;
constexpr int NSPLIT = 8, NW = 8, NTHR = 512;
constexpr int KPW = SK / NSPLIT / NW;
constexpr int NL = KPW / 4;

constexpr unsigned WS_EPOCH = 0;
constexpr unsigned WS_H1 = 4096;
constexpr unsigned H1_REC = 1152;
constexpr unsigned WS_H2 = WS_H1 + NH * NSPLIT * H1_REC;
constexpr unsigned H2_REC = 1024;
constexpr unsigned WS_END = WS_H2 + NH * NSPLIT * H2_REC;
constexpr unsigned SPIN_CAP = 1u << 16;

constexpr int L_XS = 0;
constexpr int L_RED = L_XS + NW * 32;
constexpr int L_HP = L_RED + NW * 128;
constexpr int L_FP = L_HP + NW * 64;
constexpr int L_TP = L_FP + NW * 64;
constexpr int L_MX = L_TP + NW * 64;
constexpr int L_LS = L_MX + 8;
constexpr int L_END = L_LS + 8;

__device__ __forceinline__ float asf(unsigned u) { return __builtin_bit_cast(float, u); }
__device__ __forceinline__ unsigned asu(float f) { return __builtin_bit_cast(unsigned, f); }
__device__ __forceinline__ float lo_f(unsigned u) { return asf(u << 16); }
__device__ __forceinline__ float hi_f(unsigned u) { return asf(u & 0xffff0000u); }
__device__ __forceinline__ unsigned pk_bf16(float lo, float hi) {
  bf16x2 v = {(__bf16)lo, (__bf16)hi};
  return __builtin_bit_cast(unsigned, v);
}
__device__ __forceinline__ float rbf(float x) { return (float)(__bf16)x; }
__device__ __forceinline__ float dot2(unsigned a, unsigned b, float c) {
  return __builtin_amdgcn_fdot2_f32_bf16(__builtin_bit_cast(bf16x2, a), __builtin_bit_cast(bf16x2, b), c, false);
}
template <int CTRL> __device__ __forceinline__ float dpp(float x) {
  return asf(__builtin_amdgcn_mov_dpp(__builtin_bit_cast(int, x), CTRL, 0xf, 0xf, true));
}
constexpr int XOR1 = 0xB1, XOR2 = 0x4E, XOR7 = 0x141, XOR8 = 0x128, ROR4 = 0x124, NEWBC = 0x150;
__device__ __forceinline__ float tr16(float u, float v) {
  auto s = __builtin_amdgcn_permlane16_swap(asu(u), asu(v), false, false);
  return asf(s[0]) + asf(s[1]);
}
__device__ __forceinline__ float tr32(float u, float v) {
  auto s = __builtin_amdgcn_permlane32_swap(asu(u), asu(v), false, false);
  return asf(s[0]) + asf(s[1]);
}
__device__ __forceinline__ float mx16(float u) {
  auto s = __builtin_amdgcn_permlane16_swap(asu(u), asu(u), false, false);
  return fmaxf(asf(s[0]), asf(s[1]));
}
__device__ __forceinline__ float mx32(float u) {
  auto s = __builtin_amdgcn_permlane32_swap(asu(u), asu(u), false, false);
  return fmaxf(asf(s[0]), asf(s[1]));
}
template <int CTRL> __device__ __forceinline__ float trd(float lo, float hi, bool bit) {
  const float a = lo + dpp<CTRL>(lo), b = hi + dpp<CTRL>(hi);
  return bit ? b : a;
}
__device__ __forceinline__ float wave_sum(float v) {
  v += dpp<XOR1>(v); v += dpp<XOR2>(v); v += dpp<XOR7>(v); v += dpp<XOR8>(v);
  v = tr16(v, v); v = tr32(v, v);
  return v;
}
__device__ __forceinline__ float wave_max(float v) {
  v = fmaxf(v, dpp<XOR1>(v)); v = fmaxf(v, dpp<XOR2>(v)); v = fmaxf(v, dpp<XOR7>(v)); v = fmaxf(v, dpp<XOR8>(v));
  v = mx16(v); v = mx32(v);
  return v;
}
__device__ __forceinline__ float sum_hi3(float v) {
  v += dpp<XOR8>(v); v = tr16(v, v); v = tr32(v, v);
  return v;
}
__device__ __forceinline__ float silu_f(float v) { return v / (1.0f + __expf(-v)); }
__device__ __forceinline__ v4u ld_stream(const GAS v4u* p) {
  return __builtin_nontemporal_load(p);
}
__device__ __forceinline__ void wg_barrier() {
  asm volatile("s_waitcnt lgkmcnt(0)" ::: "memory");
  __builtin_amdgcn_s_barrier();
  asm volatile("" ::: "memory");
}
#define SB() __builtin_amdgcn_sched_barrier(0)
constexpr int AUX_SC1 = 16;

__device__ __forceinline__ void row_fma(float (&acc)[8], v4u w, unsigned xlo, unsigned xhi) {
#pragma unroll
  for (int k = 0; k < 4; ++k) {
    acc[2 * k] = dot2(w[k], xlo, acc[2 * k]);
    acc[2 * k + 1] = dot2(w[k], xhi, acc[2 * k + 1]);
  }
}
__device__ __forceinline__ float tr_lo3(const float (&a)[8], int lane) {
  float y[4], z[2];
#pragma unroll
  for (int k = 0; k < 4; ++k) y[k] = trd<XOR7>(a[k], a[k + 4], lane & 4);
#pragma unroll
  for (int k = 0; k < 2; ++k) z[k] = trd<XOR2>(y[k], y[k + 2], lane & 2);
  return trd<XOR1>(z[0], z[1], lane & 1);
}

__global__ void __launch_bounds__(NTHR, 2)
fused_decode(const unsigned short* __restrict__ x_, const unsigned short* __restrict__ kc_, const unsigned short* __restrict__ vc_,
             const unsigned short* __restrict__ wq_, const unsigned short* __restrict__ wo_, const unsigned short* __restrict__ w1_,
             const unsigned short* __restrict__ w2_, unsigned short* __restrict__ out_, unsigned char* ws_) {
  __shared__ __attribute__((aligned(16))) float lds[L_END];
  const int tid = threadIdx.x, lane = tid & 63;
  const int w = __builtin_amdgcn_readfirstlane(tid >> 6);
  const int b = blockIdx.x;
  const int h = (b & 7) * 4 + (b >> 6), s = (b >> 3) & 7;
  const int r = lane >> 4, c = lane & 15, hi3 = lane >> 3, lo3 = lane & 7;
  const __amdgpu_buffer_rsrc_t rs = __builtin_amdgcn_make_buffer_rsrc((void*)ws_, 0, (int)WS_END, 0x00020000);

  const v4u xv = *(const GAS v4u*)(x_ + 8 * lane);
  const unsigned epoch = __builtin_amdgcn_raw_buffer_load_b32(rs, WS_EPOCH, 0, AUX_SC1);
  const GAS v4u* kp = (const GAS v4u*)(kc_ + ((size_t)h * SK + s * 1024 + w * KPW) * DH) + lane;
  const GAS v4u* vp = (const GAS v4u*)(vc_ + ((size_t)h * SK + s * 1024 + w * KPW) * DH) + lane;
  const GAS v4u* wqp = (const GAS v4u*)(wq_ + (size_t)(64 * w + 16 * r) * (NH * DH) + h * DH + 8 * c);
  v4u buf[NL], wq[16];
#pragma unroll
  for (int i = 0; i < 16; ++i) {
    wq[i] = wqp[i * (NH * DH / 8)];
    if (i < NPRE) buf[i] = ld_stream(kp + i * 64);
  }
#pragma unroll
  for (int i = NPRE; i < NL; ++i) buf[i] = ld_stream(kp + i * 64);
  SB();

  float xf[8];
#pragma unroll
  for (int k = 0; k < 4; ++k) { xf[2 * k] = lo_f(xv[k]); xf[2 * k + 1] = hi_f(xv[k]); }
  float sm = 0.f;
#pragma unroll
  for (int k = 0; k < 8; ++k) sm += xf[k];
  const float mean = wave_sum(sm) * (1.0f / D);
  float sq = 0.f;
#pragma unroll
  for (int k = 0; k < 8; ++k) { const float d = xf[k] - mean; sq = fmaf(d, d, sq); }
  const float var = wave_sum(sq) * (1.0f / D);
  const float mb = rbf(mean), den = rbf(sqrtf(rbf(rbf(var) + 1e-5f)));
  v4u xn;
#pragma unroll
  for (int k = 0; k < 4; ++k) xn[k] = pk_bf16(rbf(xf[2 * k] - mb) / den, rbf(xf[2 * k + 1] - mb) / den);
  LAS unsigned* ldsu = (LAS unsigned*)lds;
  LAS float* ldsf = (LAS float*)lds;
  if (hi3 == w) *(LAS v4u*)(ldsu + L_XS + w * 32 + lo3 * 4) = xn;
  asm volatile("s_waitcnt lgkmcnt(0)" ::: "memory");
  const v4u xa = *(LAS v4u*)(ldsu + L_XS + w * 32 + r * 8), xb = *(LAS v4u*)(ldsu + L_XS + w * 32 + r * 8 + 4);
  const v4u xc = *(LAS v4u*)(ldsu + L_XS + w * 32 + hi3 * 4);

  const unsigned tag = __builtin_amdgcn_readfirstlane(epoch) * 1664525u + 1013904223u;
  const unsigned h1_me = WS_H1 + (h * NSPLIT + s) * H1_REC, h2_me = WS_H2 + (s * NH + h) * H2_REC;
  {
    const v4u z = {0u, ~tag, 0u, ~tag};
    if (lane < 8) __builtin_amdgcn_raw_buffer_store_b128(z, rs, h1_me + w * 128 + lane * 16, 0, AUX_SC1);
    else if (lane < 16) __builtin_amdgcn_raw_buffer_store_b128(z, rs, h2_me + w * 128 + (lane - 8) * 16, 0, AUX_SC1);
    else if (lane == 16 && w == 0) __builtin_amdgcn_raw_buffer_store_b128(z, rs, h1_me + 1024, 0, AUX_SC1);
  }

  float acc[8];
#pragma unroll
  for (int k = 0; k < 8; ++k) acc[k] = 0.f;
#pragma unroll
  for (int i = 0; i < 16; i += 2) {
    const unsigned P = i < 8 ? xa[i >> 1] : xb[(i - 8) >> 1];
    row_fma(acc, wq[i], P & 0xffffu, P << 16);
    row_fma(acc, wq[i + 1], P >> 16, P & 0xffff0000u);
  }
  SB();
  const int g0 = 5 * h + (h < 12 ? h : 12), ng = h < 12 ? 6 : 5;
  v4u w1r[8], wor[2], w2r;
  {
    const int gg = lo3 < ng ? lo3 : ng - 1;
    const GAS v4u* w1p = (const GAS v4u*)(w1_ + (size_t)(64 * w + 8 * hi3) * FF + 8 * (g0 + gg));
#pragma unroll
    for (int i = 0; i < 8; ++i) w1r[i] = w1p[i * (FF / 8)];
    const GAS v4u* wop = (const GAS v4u*)(wo_ + (size_t)(h * DH + 16 * w + 2 * lo3) * D + 64 * s + 8 * hi3);
    wor[0] = wop[0];
    wor[1] = wop[D / 8];
    const int wg = w < ng ? w : ng - 1;
    w2r = *(const GAS v4u*)(w2_ + (size_t)(8 * (g0 + wg) + lo3) * D + 64 * s + 8 * hi3);
  }
  SB();
  {
    float y[4];
#pragma unroll
    for (int k = 0; k < 4; ++k) y[k] = tr32(acc[k], acc[k + 4]);
    const v2f z = {tr16(y[0], y[2]), tr16(y[1], y[3])};
    *(LAS v2f*)(ldsf + L_RED + w * 128 + 8 * c + 2 * r) = z;
  }
  wg_barrier();
  unsigned q[4];
  {
    const LAS v4f* qa = (const LAS v4f*)(ldsf + L_RED + (2 * r) * 128 + 8 * c);
    const v4f a0 = qa[0], a1 = qa[1], b0 = qa[32], b1 = qa[33];
    float qf[8];
#pragma unroll
    for (int k = 0; k < 4; ++k) { qf[k] = a0[k] + b0[k]; qf[4 + k] = a1[k] + b1[k]; }
#pragma unroll
    for (int k = 0; k < 8; ++k) { qf[k] = tr16(qf[k], qf[k]); qf[k] = tr32(qf[k], qf[k]); }
#pragma unroll
    for (int k = 0; k < 4; ++k) q[k] = pk_bf16(qf[2 * k], qf[2 * k + 1]);
  }
  SB();

  float sc[NL];
#pragma unroll
  for (int i = 0; i < NL; ++i) {
    float d = dot2(buf[i][0], q[0], 0.f);
    d = dot2(buf[i][1], q[1], d);
    d = dot2(buf[i][2], q[2], d);
    d = dot2(buf[i][3], q[3], d);
    sc[i] = d;
    buf[i] = ld_stream(vp + i * 64);
    SB();
  }
#pragma unroll
  for (int i = 0; i < 16; ++i) sc[i] = trd<XOR8>(sc[i], sc[i + 16], c & 8);
#pragma unroll
  for (int i = 0; i < 8; ++i) sc[i] = trd<XOR7>(sc[i], sc[i + 8], c & 4);
#pragma unroll
  for (int i = 0; i < 4; ++i) sc[i] = trd<XOR2>(sc[i], sc[i + 4], c & 2);
#pragma unroll
  for (int i = 0; i < 2; ++i) sc[i] = trd<XOR1>(sc[i], sc[i + 2], c & 1);
  constexpr float SCALE = 0.08838834764831845f;
  const float s0 = rbf(rbf(sc[0]) * SCALE), s1 = rbf(rbf(sc[1]) * SCALE);
  {
    const float wm = wave_max(fmaxf(s0, s1));
    if (lane == 0) ldsf[L_MX + w] = wm;
  }
  {
    float ha[8];
#pragma unroll
    for (int k = 0; k < 8; ++k) ha[k] = 0.f;
#pragma unroll
    for (int i = 0; i < 8; i += 2) {
      const unsigned P = xc[i >> 1];
      row_fma(ha, w1r[i], P & 0xffffu, P << 16);
      row_fma(ha, w1r[i + 1], P >> 16, P & 0xffff0000u);
    }
    float y[4], z[2];
#pragma unroll
    for (int k = 0; k < 4; ++k) y[k] = tr32(ha[k], ha[k + 4]);
#pragma unroll
    for (int k = 0; k < 2; ++k) z[k] = tr16(y[k], y[k + 2]);
    const float hv = trd<XOR8>(z[0], z[1], lane & 8);
    ldsf[L_HP + w * 64 + lo3 * 8 + hi3] = hv;
  }
  wg_barrier();
  float M;
  {
    const v4f m0 = *(const LAS v4f*)(ldsf + L_MX), m1 = *(const LAS v4f*)(ldsf + L_MX + 4);
    M = fmaxf(fmaxf(fmaxf(m0[0], m0[1]), fmaxf(m0[2], m0[3])), fmaxf(fmaxf(m1[0], m1[1]), fmaxf(m1[2], m1[3])));
  }
  constexpr float LOG2E = 1.4426950408889634f;
  float p[2];
  p[0] = __builtin_amdgcn_exp2f((s0 - M) * LOG2E);
  p[1] = __builtin_amdgcn_exp2f((s1 - M) * LOG2E);
  {
    const float ls = wave_sum(p[0] + p[1]);
    if (lane == 0) ldsf[L_LS + w] = ls;
  }
  {
    const float hp = sum_hi3(ldsf[L_HP + hi3 * 64 + w * 8 + lo3]);
    const float hb = rbf(hp);
    float hs = rbf(silu_f(hb));
    hs = w < ng ? hs : 0.f;
    float fa[8];
#pragma unroll
    for (int k = 0; k < 4; ++k) { fa[2 * k] = hs * lo_f(w2r[k]); fa[2 * k + 1] = hs * hi_f(w2r[k]); }
    ldsf[L_FP + w * 64 + lane] = tr_lo3(fa, lane);
  }
  wg_barrier();
  const float fsum = sum_hi3(ldsf[L_FP + hi3 * 64 + w * 8 + lo3]);
  if (w == 0) {
    const v4f l0 = *(const LAS v4f*)(ldsf + L_LS), l1 = *(const LAS v4f*)(ldsf + L_LS + 4);
    const float L = ((l0[0] + l0[1]) + (l0[2] + l0[3])) + ((l1[0] + l1[1]) + (l1[2] + l1[3]));
    const v4u g = {asu(M), tag, asu(L), tag};
    if (lane == 16) __builtin_amdgcn_raw_buffer_store_b128(g, rs, h1_me + 1024, 0, AUX_SC1);
  }
  float wof[16];
#pragma unroll
  for (int k = 0; k < 4; ++k) {
    wof[2 * k] = lo_f(wor[0][k]); wof[2 * k + 1] = hi_f(wor[0][k]);
    wof[8 + 2 * k] = lo_f(wor[1][k]); wof[8 + 2 * k + 1] = hi_f(wor[1][k]);
  }
  SB();

#pragma unroll
  for (int k = 0; k < 8; ++k) acc[k] = 0.f;
#define PV_STEP(i) { const float pb = dpp<NEWBC + ((i) >> 1)>(p[(i) & 1]); \
    _Pragma("unroll") for (int k = 0; k < 4; ++k) { acc[2 * k] = fmaf(pb, lo_f(buf[i][k]), acc[2 * k]); acc[2 * k + 1] = fmaf(pb, hi_f(buf[i][k]), acc[2 * k + 1]); } SB(); }
#define PV4(i) PV_STEP(i) PV_STEP(i + 1) PV_STEP(i + 2) PV_STEP(i + 3)
  PV4(0) PV4(4) PV4(8) PV4(12) PV4(16) PV4(20) PV4(24) PV4(28)
  {
    float y[4];
#pragma unroll
    for (int k = 0; k < 4; ++k) y[k] = tr32(acc[k], acc[k + 4]);
    const v2f z = {tr16(y[0], y[2]), tr16(y[1], y[3])};
    *(LAS v2f*)(ldsf + L_RED + w * 128 + 8 * c + 2 * r) = z;
  }
  wg_barrier();
  {
    const v2f z = *(const LAS v2f*)(ldsf + L_RED + hi3 * 128 + 16 * w + 2 * lo3);
    const float o0 = sum_hi3(z[0]), o1 = sum_hi3(z[1]);
    const v4u g = {asu(o0), tag, asu(o1), tag};
    if (lane < 8) __builtin_amdgcn_raw_buffer_store_b128(g, rs, h1_me + w * 128 + lane * 16, 0, AUX_SC1);
  }
  bool alive = true;
  {
    const unsigned base = WS_H1 + (h * NSPLIT + hi3) * H1_REC;
    v4u go, gm;
 for (unsigned spins = 0;;) {
      asm volatile("" ::: "memory");
      go = __builtin_amdgcn_raw_buffer_load_b128(rs, base + w * 128 + lo3 * 16, 0, AUX_SC1);
      gm = __builtin_amdgcn_raw_buffer_load_b128(rs, base + 1024, 0, AUX_SC1);
      const bool ok = go[1] == tag && go[3] == tag && gm[1] == tag && gm[3] == tag;
      if (__all(ok)) break;
      if (++spins > SPIN_CAP) { alive = false; break; }
      __builtin_amdgcn_s_sleep(1);
    }
    const float mj = asf(gm[0]), lj = asf(gm[2]);
    float Mh = fmaxf(mj, dpp<XOR8>(mj));
    Mh = mx32(mx16(Mh));
    const float wt = __builtin_amdgcn_exp2f((mj - Mh) * LOG2E);
    const float Lh = sum_hi3(wt * lj);
    const float o0 = rbf(sum_hi3(wt * asf(go[0])) / Lh), o1 = rbf(sum_hi3(wt * asf(go[2])) / Lh);
    float ta[8];
#pragma unroll
    for (int k = 0; k < 8; ++k) ta[k] = fmaf(o0, wof[k], o1 * wof[8 + k]);
    ldsf[L_TP + w * 64 + lane] = tr_lo3(ta, lane);
  }
  wg_barrier();
  {
    float t = sum_hi3(ldsf[L_TP + hi3 * 64 + w * 8 + lo3]);
    if (!alive) t = asf(0x7fc00000u);
    const v4u g = {asu(t), tag, asu(fsum), tag};
    if (lane >= 8 && lane < 16) __builtin_amdgcn_raw_buffer_store_b128(g, rs, h2_me + w * 128 + (lane - 8) * 16, 0, AUX_SC1);
  }
  if (h == 4 * s) {
    const unsigned base = WS_H2 + (s * NH + hi3) * H2_REC + w * 128 + lo3 * 16;
    const float xres = lo_f((unsigned)x_[64 * s + 8 * w + lo3]);
    v4u g[4];
    for (unsigned spins = 0;;) {
      asm volatile("" ::: "memory");
      bool ok = true;
#pragma unroll
      for (int i = 0; i < 4; ++i) g[i] = __builtin_amdgcn_raw_buffer_load_b128(rs, base + i * 8 * H2_REC, 0, AUX_SC1);
#pragma unroll
      for (int i = 0; i < 4; ++i) ok = ok && g[i][1] == tag && g[i][3] == tag;
      if (__all(ok)) break;
      if (++spins > SPIN_CAP) { alive = false; break; }
      __builtin_amdgcn_s_sleep(1);
    }
    const float st = sum_hi3((asf(g[0][0]) + asf(g[1][0])) + (asf(g[2][0]) + asf(g[3][0])));
    const float sf = sum_hi3((asf(g[0][2]) + asf(g[1][2])) + (asf(g[2][2]) + asf(g[3][2])));
    const float gate = rbf(silu_f(rbf(sf)));
    float o = xres + rbf(rbf(st) * gate);
    if (!alive) o = asf(0x7fc00000u);
    if (lane < 8) out_[64 * s + 8 * w + lane] = (unsigned short)(pk_bf16(o, o) & 0xffffu);
    if (s == 0 && w == 0 && lane == 0) __builtin_amdgcn_raw_buffer_store_b32(tag, rs, WS_EPOCH, 0, AUX_SC1);
  }
}

}

extern "C" void kernel_launch(void* const* d_in, const int* in_sizes, int n_in, void* d_out, int out_size, void* d_ws,
                              size_t ws_size, hipStream_t stream) {
  (void)in_sizes; (void)n_in; (void)out_size; (void)ws_size;
  fused_decode<<<dim3(NH * NSPLIT), dim3(NTHR), 0, stream>>>(
      (const unsigned short*)d_in[0], (const unsigned short*)d_in[1], (const unsigned short*)d_in[2],
      (const unsigned short*)d_in[3], (const unsigned short*)d_in[4], (const unsigned short*)d_in[6],
      (const unsigned short*)d_in[7], (unsigned short*)d_out, (unsigned char*)d_ws);
}
```

```cpp
#include <hip/hip_runtime.h>
#include <hip/hip_bf16.h>
#include <stdint.h>

#define GAS __attribute__((address_space(1)))
#define LAS __attribute__((address_space(3)))
typedef unsigned v4u __attribute__((ext_vector_type(4)));
typedef float v4f __attribute__((ext_vector_type(4)));
typedef float v2f __attribute__((ext_vector_type(2)));
typedef __bf16 bf16x2 __attribute__((ext_vector_type(2)));

#define WK 6
#define WV 8
#define SVC_SLP 8
#define W_AUX 2

#define STAMP(k) do {} while (0)
#define STAMP_OUT() do {} while (0)

namespace {

constexpr int D = 512, NH = 32, SK = 8192, DH = 128, FF = 1376;
constexpr int NSPLIT = 8, NSW = 8, NTHR = 64 * (NSW + 1);
constexpr int NL = SK / NSPLIT / NSW / 4;

constexpr unsigned WS_EPOCH = 0;
constexpr unsigned WS_X1 = 4096, X1_REC = 2048;
constexpr unsigned WS_X2 = WS_X1 + 256 * X1_REC, X2_REC = 128;
constexpr unsigned WS_X3 = WS_X2 + 256 * X2_REC, X3_REC = 1024;
constexpr unsigned WS_H1 = WS_X3 + 256 * X3_REC, H1_REC = 1024;
constexpr unsigned WS_H2 = WS_H1 + 256 * H1_REC, H2_REC = 512;
constexpr unsigned WS_END = WS_H2 + 256 * H2_REC;
constexpr unsigned SPIN_CAP = 1u << 16;

constexpr int L_RED = 0;
constexpr int L_TP = L_RED + NSW * 128;
constexpr int L_XS = L_TP + NSW * 64;
constexpr int L_XR = L_XS + 32;
constexpr int L_Q = L_XR + 32;
constexpr int L_GATE = L_Q + 64;
constexpr int L_ML = L_GATE + 64;
constexpr int L_F = L_ML + 16;
constexpr int L_FLAG = L_F + 8;
constexpr int L_END = L_FLAG + 4;

__device__ __forceinline__ float asf(unsigned u) { return __builtin_bit_cast(float, u); }
__device__ __forceinline__ unsigned asu(float f) { return __builtin_bit_cast(unsigned, f); }
__device__ __forceinline__ float lo_f(unsigned u) { return asf(u << 16); }
__device__ __forceinline__ float hi_f(unsigned u) { return asf(u & 0xffff0000u); }
__device__ __forceinline__ unsigned pk_bf16(float lo, float hi) {
  bf16x2 v = {(__bf16)lo, (__bf16)hi};
  return __builtin_bit_cast(unsigned, v);
}
__device__ __forceinline__ float rbf(float x) { return (float)(__bf16)x; }
__device__ __forceinline__ float dot2(unsigned a, unsigned b, float c) {
  return __builtin_amdgcn_fdot2_f32_bf16(__builtin_bit_cast(bf16x2, a), __builtin_bit_cast(bf16x2, b), c, false);
}
template <int CTRL> __device__ __forceinline__ float dpp(float x) {
  return asf(__builtin_amdgcn_mov_dpp(__builtin_bit_cast(int, x), CTRL, 0xf, 0xf, true));
}
constexpr int XOR1 = 0xB1, XOR2 = 0x4E, XOR7 = 0x141, XOR8 = 0x128, NEWBC = 0x150;
__device__ __forceinline__ float tr16(float u, float v) {
  auto s = __builtin_amdgcn_permlane16_swap(asu(u), asu(v), false, false);
  return asf(s[0]) + asf(s[1]);
}
__device__ __forceinline__ float tr32(float u, float v) {
  auto s = __builtin_amdgcn_permlane32_swap(asu(u), asu(v), false, false);
  return asf(s[0]) + asf(s[1]);
}
__device__ __forceinline__ float mx16(float u) {
  auto s = __builtin_amdgcn_permlane16_swap(asu(u), asu(u), false, false);
  return fmaxf(asf(s[0]), asf(s[1]));
}
__device__ __forceinline__ float mx32(float u) {
  auto s = __builtin_amdgcn_permlane32_swap(asu(u), asu(u), false, false);
  return fmaxf(asf(s[0]), asf(s[1]));
}
template <int CTRL> __device__ __forceinline__ float trd(float lo, float hi, bool bit) {
  const float a = lo + dpp<CTRL>(lo), b = hi + dpp<CTRL>(hi);
  return bit ? b : a;
}
__device__ __forceinline__ float sum_lo3(float v) { v += dpp<XOR1>(v); v += dpp<XOR2>(v); v += dpp<XOR7>(v); return v; }
__device__ __forceinline__ float max_lo3(float v) { v = fmaxf(v, dpp<XOR1>(v)); v = fmaxf(v, dpp<XOR2>(v)); v = fmaxf(v, dpp<XOR7>(v)); return v; }
__device__ __forceinline__ float sum_hi3(float v) { v += dpp<XOR8>(v); v = tr16(v, v); v = tr32(v, v); return v; }
__device__ __forceinline__ float wave_sum(float v) { return sum_hi3(sum_lo3(v)); }
__device__ __forceinline__ float wave_max(float v) { v = max_lo3(v); v = fmaxf(v, dpp<XOR8>(v)); return mx32(mx16(v)); }
__device__ __forceinline__ float silu_f(float v) { return v / (1.0f + __expf(-v)); }
__device__ __forceinline__ void wg_barrier() {
  asm volatile("s_waitcnt lgkmcnt(0)" ::: "memory");
  __builtin_amdgcn_s_barrier();
  asm volatile("" ::: "memory");
}
#define SB() __builtin_amdgcn_sched_barrier(0)
#define VMWAIT(n) asm volatile("s_waitcnt vmcnt(%0)" ::"i"(n) : "memory")
constexpr int AUX_NT = 2, AUX_SC1 = 16;
typedef __amdgpu_buffer_rsrc_t rsrc_t;
__device__ __forceinline__ rsrc_t mk_rsrc(const void* p, unsigned bytes) { return __builtin_amdgcn_make_buffer_rsrc((void*)p, 0, (int)bytes, 0x00020000); }
template <int AUX> __device__ __forceinline__ v4u ld16(rsrc_t r, unsigned voff, unsigned soff) { return __builtin_amdgcn_raw_buffer_load_b128(r, voff, soff, AUX); }
__device__ __forceinline__ void put_cell(rsrc_t r, unsigned off, float a, float b, unsigned tag) {
  const v4u g = {asu(a), tag, asu(b), tag};
  __builtin_amdgcn_raw_buffer_store_b128(g, r, off, 0, AUX_SC1);
}
template <int N, unsigned STR, int SLP = 1> __device__ __forceinline__ void get_cells(rsrc_t r, unsigned off, unsigned tag, v4u (&g)[N]) {
  for (unsigned spins = 0;;) {
    asm volatile("" ::: "memory");
#pragma unroll
    for (int i = 0; i < N; ++i) g[i] = ld16<AUX_SC1>(r, off, i * STR);
    bool ok = true;
#pragma unroll
    for (int i = 0; i < N; ++i) ok = ok & (g[i][1] == tag) & (g[i][3] == tag);
    if (__all(ok)) break;
    if (++spins > SPIN_CAP) {
#pragma unroll
      for (int i = 0; i < N; ++i) g[i][0] = g[i][2] = 0x7fc00000u;
      break;
    }
    __builtin_amdgcn_s_sleep(SLP);
  }
}
__device__ __forceinline__ void lds_wait(LAS unsigned* p, unsigned want) {
  for (unsigned spins = 0; spins < (SPIN_CAP << 6); ++spins) {
    if (__hip_atomic_load(p, __ATOMIC_RELAXED, __HIP_MEMORY_SCOPE_WORKGROUP) >= want) break;
    __builtin_amdgcn_s_sleep(1);
  }
  asm volatile("" ::: "memory");
}
__device__ __forceinline__ void lds_post(LAS unsigned* p) {
  asm volatile("s_waitcnt lgkmcnt(0)" ::: "memory");
  if ((threadIdx.x & 63) == 0) __hip_atomic_fetch_add(p, 1u, __ATOMIC_RELAXED, __HIP_MEMORY_SCOPE_WORKGROUP);
}
__device__ __forceinline__ void row_fma(float (&acc)[8], v4u w, unsigned xlo, unsigned xhi) {
#pragma unroll
  for (int k = 0; k < 4; ++k) {
    acc[2 * k] = dot2(w[k], xlo, acc[2 * k]);
    acc[2 * k + 1] = dot2(w[k], xhi, acc[2 * k + 1]);
  }
}
__device__ __forceinline__ float tr_lo3(const float (&a)[8], int lane) {
  float y[4], z[2];
#pragma unroll
  for (int k = 0; k < 4; ++k) y[k] = trd<XOR7>(a[k], a[k + 4], lane & 4);
#pragma unroll
  for (int k = 0; k < 2; ++k) z[k] = trd<XOR2>(y[k], y[k + 2], lane & 2);
  return trd<XOR1>(z[0], z[1], lane & 1);
}
__device__ __forceinline__ float tr_hi3(const float (&a)[8], int lane) {
  float y[4], z[2];
#pragma unroll
  for (int k = 0; k < 4; ++k) y[k] = tr32(a[k], a[k + 4]);
#pragma unroll
  for (int k = 0; k < 2; ++k) z[k] = tr16(y[k], y[k + 2]);
  return trd<XOR8>(z[0], z[1], lane & 8);
}
constexpr int brev4(int i) { return ((i & 1) << 3) | ((i & 2) << 1) | ((i & 4) >> 1) | ((i & 8) >> 3); }
constexpr float LOG2E = 1.4426950408889634f;

struct KState { float l0, l1, l2, l3, sc[2]; };
template <int I> __device__ __forceinline__ void k_dot(KState& st, const v4u& k, const v4u& q, int c) {
  float d = dot2(k[0], q[0], 0.f);
  d = dot2(k[1], q[1], d);
  d = dot2(k[2], q[2], d);
  d = dot2(k[3], q[3], d);
  if constexpr ((I & 1) == 0) { st.l0 = d; return; }
  d = trd<XOR8>(st.l0, d, c & 8);
  if constexpr ((I & 2) == 0) { st.l1 = d; return; }
  d = trd<XOR7>(st.l1, d, c & 4);
  if constexpr ((I & 4) == 0) { st.l2 = d; return; }
  d = trd<XOR2>(st.l2, d, c & 2);
  if constexpr ((I & 8) == 0) { st.l3 = d; return; }
  st.sc[I >> 4] = trd<XOR1>(st.l3, d, c & 1);
}
template <int LO, int HI> __device__ __forceinline__ void k_dots(KState& st, const v4u (&buf)[NL], const v4u& q, int c) {
  if constexpr (LO < HI && LO < NL) { k_dot<LO>(st, buf[LO], q, c); k_dots<LO + 1, HI>(st, buf, q, c); }
}
template <int I> __device__ __forceinline__ void pv_step(v2f (&acc)[4], const v4u& v, const float (&p)[2]) {
  const float pb = dpp<NEWBC + brev4(I & 15)>(p[I >> 4]);
  const v2f pp = {pb, pb};
#pragma unroll
  for (int k = 0; k < 4; ++k) { const v2f vf = {lo_f(v[k]), hi_f(v[k])}; acc[k] = __builtin_elementwise_fma(pp, vf, acc[k]); }
  asm volatile("" : "+v"(acc[0]), "+v"(acc[1]), "+v"(acc[2]), "+v"(acc[3]));
}
constexpr int KB = (NL - WK + WV - 1) / WV;
template <int J> __device__ __forceinline__ void phase_b(KState& st, v4u (&buf)[NL], const v4u& q, int c, rsrc_t rv, unsigned voff) {
  if constexpr (J < WV) {
    k_dots<J * KB, (J + 1) * KB < NL - WK ? (J + 1) * KB : NL - WK>(st, buf, q, c);
    SB();
    buf[J] = ld16<AUX_NT>(rv, voff, J * 8192);
    SB();
    phase_b<J + 1>(st, buf, q, c, rv, voff);
  }
}
template <int J, int END> __device__ __forceinline__ void phase_c(v2f (&acc)[4], v4u (&buf)[NL], const float (&p)[2], rsrc_t rv, unsigned voff) {
  if constexpr (J < END) {
    pv_step<J>(acc, buf[J], p);
    SB();
    if constexpr (J + WV < NL) { buf[J + WV] = ld16<AUX_NT>(rv, voff, (J + WV) * 8192); SB(); }
    phase_c<J + 1, END>(acc, buf, p, rv, voff);
  }
}

__global__ void __launch_bounds__(NTHR, 3)
fused_decode(const unsigned short* __restrict__ x_, const unsigned short* __restrict__ kc_, const unsigned short* __restrict__ vc_,
             const unsigned short* __restrict__ wq_, const unsigned short* __restrict__ wo_, const unsigned short* __restrict__ w1_,
             const unsigned short* __restrict__ w2_, unsigned short* __restrict__ out_, unsigned char* ws_) {
  __shared__ __attribute__((aligned(16))) unsigned lds[L_END];
  LAS unsigned* ldsu = (LAS unsigned*)lds;
  LAS float* ldsf = (LAS float*)lds;
  const int tid = threadIdx.x, lane = tid & 63;
  const int w = __builtin_amdgcn_readfirstlane(tid >> 6);
  const int b = blockIdx.x;
  const int h = (b & 7) * 4 + (b >> 6), s = (b >> 3) & 7;
  const int r = lane >> 4, c = lane & 15, hi3 = lane >> 3, lo3 = lane & 7;
  const rsrc_t rs = mk_rsrc(ws_, WS_END);
  STAMP(0);

  if (w == 0) {
    __builtin_amdgcn_s_setprio(3);
    const int g0 = 5 * h + (h < 12 ? h : 12), ng = h < 12 ? 6 : 5;
    v4u xv = ld16<0>(mk_rsrc(x_, D * 2), lane * 16, 0);
    const unsigned epoch = __builtin_amdgcn_raw_buffer_load_b32(rs, WS_EPOCH, 0, AUX_SC1);
    SB();
    v4u wq[16], w1r[8], w2r[6];
    {
      const rsrc_t rq = mk_rsrc(wq_, D * NH * DH * 2), r1 = mk_rsrc(w1_, D * FF * 2), r2 = mk_rsrc(w2_, FF * D * 2);
      const unsigned oq = ((64 * s + 16 * r) * (NH * DH) + h * DH + 8 * c) * 2;
#pragma unroll
      for (int i = 0; i < 16; ++i) wq[i] = ld16<W_AUX>(rq, oq, i * (NH * DH * 2));
      SB();
      const unsigned o1 = ((64 * s + 8 * hi3) * FF + 8 * (g0 + (lo3 < ng ? lo3 : ng - 1))) * 2;
#pragma unroll
      for (int i = 0; i < 8; ++i) w1r[i] = ld16<W_AUX>(r1, o1, i * (FF * 2));
      const unsigned o2 = ((8 * g0 + hi3) * D + 64 * s + 8 * lo3) * 2;
#pragma unroll
      for (int j = 0; j < 6; ++j) w2r[j] = ld16<W_AUX>(r2, o2, (j < ng ? j : ng - 1) * (8 * D * 2));
    }
    if (lane < 4) ldsu[L_FLAG + lane] = 0u;
    SB();
    wg_barrier();
    SB();
    asm volatile("" : "+v"(xv));
    float xf[8];
#pragma unroll
    for (int k = 0; k < 4; ++k) { xf[2 * k] = lo_f(xv[k]); xf[2 * k + 1] = hi_f(xv[k]); }
    float sm = 0.f;
#pragma unroll
    for (int k = 0; k < 8; ++k) sm += xf[k];
    const float mean = wave_sum(sm) * (1.0f / D);
    float sq = 0.f;
#pragma unroll
    for (int k = 0; k < 8; ++k) { const float d = xf[k] - mean; sq = fmaf(d, d, sq); }
    const float var = wave_sum(sq) * (1.0f / D);
    const float mb = rbf(mean), den = rbf(sqrtf(rbf(rbf(var) + 1e-5f)));
    v4u xn;
#pragma unroll
    for (int k = 0; k < 4; ++k) xn[k] = pk_bf16(rbf(xf[2 * k] - mb) / den, rbf(xf[2 * k + 1] - mb) / den);
    if (hi3 == s) { *(LAS v4u*)(ldsu + L_XS + lo3 * 4) = xn; *(LAS v4u*)(ldsu + L_XR + lo3 * 4) = xv; }
    asm volatile("s_waitcnt lgkmcnt(0)" ::: "memory");
    const v4u xa = *(LAS v4u*)(ldsu + L_XS + r * 8), xb = *(LAS v4u*)(ldsu + L_XS + r * 8 + 4), xc = *(LAS v4u*)(ldsu + L_XS + hi3 * 4);
    const unsigned tag = __builtin_amdgcn_readfirstlane(epoch) * 1664525u + 1013904223u;
    if (lane == 0) ldsu[L_FLAG + 3] = tag;
    STAMP(1);
    const unsigned x1_me = WS_X1 + (h * NSPLIT + s) * X1_REC, pairq = (4 * c + r) * 16;
    {
      float acc[8];
#pragma unroll
      for (int k = 0; k < 8; ++k) acc[k] = 0.f;
#pragma unroll
      for (int i = 0; i < 16; i += 2) {
        const unsigned P = i < 8 ? xa[i >> 1] : xb[(i - 8) >> 1];
        row_fma(acc, wq[i], P & 0xffffu, P << 16);
        row_fma(acc, wq[i + 1], P >> 16, P & 0xffff0000u);
      }
      float y[4];
#pragma unroll
      for (int k = 0; k < 4; ++k) y[k] = tr32(acc[k], acc[k + 4]);
      put_cell(rs, x1_me + pairq, tr16(y[0], y[2]), tr16(y[1], y[3]), tag);
#pragma unroll
      for (int k = 0; k < 8; ++k) acc[k] = 0.f;
#pragma unroll
      for (int i = 0; i < 8; i += 2) {
        const unsigned P = xc[i >> 1];
        row_fma(acc, w1r[i], P & 0xffffu, P << 16);
        row_fma(acc, w1r[i + 1], P >> 16, P & 0xffff0000u);
      }
      const float hv = tr_hi3(acc, lane);
      put_cell(rs, x1_me + 1024 + lane * 16, hv, hv, tag);
    }
    STAMP(2);
    {
      v4u g[8];
      get_cells<8, X1_REC, SVC_SLP>(rs, WS_X1 + h * NSPLIT * X1_REC + lane * 16, tag, g);
      float q0 = 0.f, q1 = 0.f;
#pragma unroll
      for (int i = 0; i < 8; ++i) { q0 += asf(g[i][0]); q1 += asf(g[i][2]); }
      ldsu[L_Q + lane] = pk_bf16(q0, q1);
      lds_post(ldsu + L_FLAG + 0);
    }
    STAMP(3);
    {
      v4u g[8];
      get_cells<8, X1_REC, SVC_SLP>(rs, WS_X1 + h * NSPLIT * X1_REC + 1024 + lane * 16, tag, g);
      float hp = 0.f;
#pragma unroll
      for (int i = 0; i < 8; ++i) hp += asf(g[i][0]);
      float hs = rbf(silu_f(rbf(hp)));
      hs = lo3 < ng ? hs : 0.f;
      float fa[8];
#pragma unroll
      for (int k = 0; k < 8; ++k) fa[k] = 0.f;
#define FFN2(j) { const float hj = asf(__builtin_amdgcn_ds_swizzle(asu(hs), 0x18 | ((j) << 5))); \
      _Pragma("unroll") for (int k = 0; k < 4; ++k) { fa[2 * k] = fmaf(hj, lo_f(w2r[j][k]), fa[2 * k]); fa[2 * k + 1] = fmaf(hj, hi_f(w2r[j][k]), fa[2 * k + 1]); } }
      FFN2(0) FFN2(1) FFN2(2) FFN2(3) FFN2(4) FFN2(5)
      const float fv = tr_hi3(fa, lane);
      put_cell(rs, WS_X3 + (s * NH + h) * X3_REC + (8 * lo3 + hi3) * 16, fv, fv, tag);
    }
    STAMP(4);
    if (h == 4 * s) {
      float f = 0.f;
#pragma unroll
      for (int half = 0; half < 2; ++half) {
        v4u g[16];
        get_cells<16, X3_REC, SVC_SLP>(rs, WS_X3 + (s * NH + 16 * half) * X3_REC + lane * 16, tag, g);
#pragma unroll
        for (int i = 0; i < 16; ++i) f += asf(g[i][0]);
      }
      ldsf[L_GATE + lane] = rbf(silu_f(rbf(f)));
    }
    STAMP(5);
    lds_wait(ldsu + L_FLAG + 1, NSW);
    STAMP(6);
    {
      const v2f ml = *(LAS v2f*)(ldsf + L_ML + 2 * lo3);
      const float M = max_lo3(ml[0]);
      const float L = sum_lo3(ml[1] * __builtin_amdgcn_exp2f((ml[0] - M) * LOG2E));
      if (lane == 0) put_cell(rs, WS_X2 + (h * NSPLIT + s) * X2_REC, M, L, tag);
      v4u g[1];
      get_cells<1, 0, SVC_SLP>(rs, WS_X2 + (h * NSPLIT + lo3) * X2_REC, tag, g);
      const float Mh = max_lo3(asf(g[0][0]));
      const float Lh = sum_lo3(asf(g[0][2]) * __builtin_amdgcn_exp2f((asf(g[0][0]) - Mh) * LOG2E));
      if (lane < 8) ldsf[L_F + lane] = __builtin_amdgcn_exp2f((ml[0] - Mh) * LOG2E) / Lh;
      lds_post(ldsu + L_FLAG + 2);
    }
    STAMP(7);
    STAMP_OUT();
    return;
  }

  const int sw = w - 1;
  const size_t kvoff = ((size_t)h * SK + s * 1024) * DH;
  const rsrc_t rk = mk_rsrc(kc_ + kvoff, 1024 * DH * 2), rv = mk_rsrc(vc_ + kvoff, 1024 * DH * 2);
  const unsigned voff = sw * 1024 + lane * 16;
  v4u buf[NL], wor[2];
#pragma unroll
  for (int i = 0; i < WK; ++i) buf[i] = ld16<AUX_NT>(rk, voff, i * 8192);
  SB();
  wg_barrier();
#pragma unroll
  for (int i = WK; i < NL; ++i) {
    VMWAIT(WK - 1);
    buf[i] = ld16<AUX_NT>(rk, voff, i * 8192);
    if (i == 8) {
      const rsrc_t ro = mk_rsrc(wo_, NH * DH * D * 2);
      const unsigned oo = ((h * DH + 16 * sw + 2 * lo3) * D + 64 * s + 8 * hi3) * 2;
      wor[0] = ld16<W_AUX>(ro, oo, 0);
      wor[1] = ld16<W_AUX>(ro, oo, D * 2);
    }
    SB();
  }
  STAMP(1);
  lds_wait(ldsu + L_FLAG + 0, 1);
  const v4u q = *(LAS v4u*)(ldsu + L_Q + 4 * c);
  const unsigned tag = __builtin_amdgcn_readfirstlane(ldsu[L_FLAG + 3]);
  STAMP(2);
  KState st;
  phase_b<0>(st, buf, q, c, rv, voff);
  k_dots<NL - WK, NL>(st, buf, q, c);
  STAMP(3);
  constexpr float SCALE = 0.08838834764831845f;
  const float s0 = rbf(rbf(st.sc[0]) * SCALE), s1 = rbf(rbf(st.sc[1]) * SCALE);
  const float m = wave_max(fmaxf(s0, s1));
  float p[2];
  p[0] = __builtin_amdgcn_exp2f((s0 - m) * LOG2E);
  p[1] = __builtin_amdgcn_exp2f((s1 - m) * LOG2E);
  {
    const v2f ml = {m, wave_sum(p[0] + p[1])};
    if (lane == 0) *(LAS v2f*)(ldsf + L_ML + 2 * sw) = ml;
    lds_post(ldsu + L_FLAG + 1);
  }
  float wof[16];
#pragma unroll
  for (int k = 0; k < 4; ++k) {
    wof[2 * k] = lo_f(wor[0][k]); wof[2 * k + 1] = hi_f(wor[0][k]);
    wof[8 + 2 * k] = lo_f(wor[1][k]); wof[8 + 2 * k + 1] = hi_f(wor[1][k]);
  }
#pragma unroll
  for (int k = 0; k < 16; ++k) asm volatile("" : "+v"(wof[k]));
  STAMP(4);
  v2f acc[4];
#pragma unroll
  for (int k = 0; k < 4; ++k) acc[k] = v2f{0.f, 0.f};
  phase_c<0, NL - 3>(acc, buf, p, rv, voff);
  lds_wait(ldsu + L_FLAG + 2, 1);
  const float fw = ldsf[L_F + sw];
  const bool fin = h == 4 * s;
  const v2f gt = *(LAS v2f*)(ldsf + L_GATE + 8 * sw + 2 * (lane & 3));
  const unsigned xx = ldsu[L_XR + 4 * sw + (lane & 3)];
  float gate0 = gt[0], gate1 = gt[1], x0 = lo_f(xx), x1 = hi_f(xx);
  asm volatile("" : "+v"(gate0), "+v"(gate1), "+v"(x0), "+v"(x1));
  STAMP(5);
  phase_c<NL - 3, NL>(acc, buf, p, rv, voff);
  STAMP(6);
  {
    const float y0 = tr32(acc[0][0], acc[2][0]), y1 = tr32(acc[0][1], acc[2][1]), y2 = tr32(acc[1][0], acc[3][0]), y3 = tr32(acc[1][1], acc[3][1]);
    const v2f z = {tr16(y0, y2) * fw, tr16(y1, y3) * fw};
    *(LAS v2f*)(ldsf + L_RED + sw * 128 + 8 * c + 2 * r) = z;
  }
  wg_barrier();
  {
    const v2f z = *(const LAS v2f*)(ldsf + L_RED + hi3 * 128 + 16 * sw + 2 * lo3);
    const float o0 = sum_hi3(z[0]), o1 = sum_hi3(z[1]);
    if (lane < 8) put_cell(rs, WS_H1 + (h * NSPLIT + s) * H1_REC + sw * 128 + lane * 16, o0, o1, tag);
    v4u g[1];
    get_cells<1, 0>(rs, WS_H1 + (h * NSPLIT + hi3) * H1_REC + sw * 128 + lo3 * 16, tag, g);
    STAMP(7);
    const float a0 = rbf(sum_hi3(asf(g[0][0]))), a1 = rbf(sum_hi3(asf(g[0][2])));
    float ta[8];
#pragma unroll
    for (int k = 0; k < 8; ++k) ta[k] = fmaf(a0, wof[k], a1 * wof[8 + k]);
    ldsf[L_TP + sw * 64 + lane] = tr_lo3(ta, lane);
  }
  wg_barrier();
  {
    const v2f z = *(const LAS v2f*)(ldsf + L_TP + ((lane >> 2) & 7) * 64 + 8 * sw + 2 * (lane & 3));
    float t0 = z[0], t1 = z[1];
    t0 += dpp<0x124>(t0); t1 += dpp<0x124>(t1);
    t0 += dpp<XOR8>(t0); t1 += dpp<XOR8>(t1);
    t0 = tr16(t0, t0); t1 = tr16(t1, t1);
    if (lane < 4) put_cell(rs, WS_H2 + (s * NH + h) * H2_REC + sw * 64 + lane * 16, t0, t1, tag);
  }
  STAMP(8);
  if (fin) {
    v4u g[2];
    get_cells<2, 16 * H2_REC>(rs, WS_H2 + (s * NH + (lane >> 2)) * H2_REC + sw * 64 + (lane & 3) * 16, tag, g);
    float t0 = asf(g[0][0]) + asf(g[1][0]), t1 = asf(g[0][2]) + asf(g[1][2]);
    t0 += dpp<0x124>(t0); t1 += dpp<0x124>(t1);
    t0 = sum_hi3(t0); t1 = sum_hi3(t1);
    const float o0 = x0 + rbf(rbf(t0) * gate0), o1 = x1 + rbf(rbf(t1) * gate1);
    if (lane < 4) ((unsigned*)out_)[32 * s + 4 * sw + lane] = pk_bf16(o0, o1);
    if (s == 0 && sw == 0 && lane == 0) __builtin_amdgcn_raw_buffer_store_b32(tag, rs, WS_EPOCH, 0, AUX_SC1);
    STAMP(9);
  }
  STAMP_OUT();
}

}

extern "C" void kernel_launch(void* const* d_in, const int* in_sizes, int n_in, void* d_out, int out_size, void* d_ws,
                              size_t ws_size, hipStream_t stream) {
  (void)in_sizes; (void)n_in; (void)out_size; (void)ws_size;
  fused_decode<<<dim3(NH * NSPLIT), dim3(NTHR), 0, stream>>>(
      (const unsigned short*)d_in[0], (const unsigned short*)d_in[1], (const unsigned short*)d_in[2],
      (const unsigned short*)d_in[3], (const unsigned short*)d_in[4], (const unsigned short*)d_in[6],
      (const unsigned short*)d_in[7], (unsigned short*)d_out, (unsigned char*)d_ws);
}
```

```cpp
#include <hip/hip_runtime.h>
#include <stdint.h>

#define LAS __attribute__((address_space(3)))
typedef unsigned v4u __attribute__((ext_vector_type(4)));
typedef float v2f __attribute__((ext_vector_type(2)));
typedef __bf16 bf16x2 __attribute__((ext_vector_type(2)));

namespace {

constexpr int WIN = 6, WIN0 = 3, PSTART = 10;
constexpr int PA = 55, PD = 460;
constexpr int QS = 26, DPS = 5;
constexpr int PRET = 46, X2SLOT = 50;
constexpr int X1DELAY = 32, C1 = 120, D1 = 4, D2 = 6;
constexpr unsigned FINX = 0;

constexpr int D = 512, NH = 32, SK = 8192, DH = 128, FF = 1376;
constexpr int NSPLIT = 8, NSW = 8, NTHR = 64 * (NSW + 1);
constexpr int NL = SK / NSPLIT / NSW / 4;

constexpr unsigned WS_EPOCH = 0;
constexpr unsigned WS_T = 1024;
constexpr unsigned WS_X1 = 4096, X1_REC = 1536;
constexpr unsigned WS_X2 = WS_X1 + 256 * X1_REC, X2_REC = 128;
constexpr unsigned WS_H1 = WS_X2 + 256 * X2_REC, H1_REC = 1024;
constexpr unsigned WS_H2 = WS_H1 + 256 * H1_REC, H2_REC = 1024;
constexpr unsigned WS_END = WS_H2 + 256 * H2_REC;
constexpr unsigned SPIN_CAP = 1u << 16, OOB = 0x7fffffffu;

constexpr int L_RED = 0;
constexpr int L_TP = L_RED + NSW * 128;
constexpr int L_FV = L_TP + NSW * 64;
constexpr int L_XS = L_FV + 64;
constexpr int L_XR = L_XS + 32;
constexpr int L_Q = L_XR + 8;
constexpr int L_ML = L_Q + 64;
constexpr int L_F = L_ML + 16;
constexpr int L_FLAG = L_F + 8;
constexpr int F_Q = L_FLAG, F_ML = L_FLAG + 1, F_F = L_FLAG + 2, F_TAG = L_FLAG + 3, F_PROG = L_FLAG + 4, F_TB = L_FLAG + 5, F_FIN = L_FLAG + 6;
constexpr int L_END = L_FLAG + 8;

__device__ __forceinline__ float asf(unsigned u) { return __builtin_bit_cast(float, u); }
__device__ __forceinline__ unsigned asu(float f) { return __builtin_bit_cast(unsigned, f); }
__device__ __forceinline__ float lo_f(unsigned u) { return asf(u << 16); }
__device__ __forceinline__ float hi_f(unsigned u) { return asf(u & 0xffff0000u); }
__device__ __forceinline__ unsigned pk_bf16(float lo, float hi) {
  bf16x2 v = {(__bf16)lo, (__bf16)hi};
  return __builtin_bit_cast(unsigned, v);
}
__device__ __forceinline__ float rbf(float x) { return (float)(__bf16)x; }
__device__ __forceinline__ float dot2(unsigned a, unsigned b, float c) {
  return __builtin_amdgcn_fdot2_f32_bf16(__builtin_bit_cast(bf16x2, a), __builtin_bit_cast(bf16x2, b), c, false);
}
template <int CTRL> __device__ __forceinline__ float dpp(float x) {
  return asf(__builtin_amdgcn_mov_dpp(__builtin_bit_cast(int, x), CTRL, 0xf, 0xf, true));
}
constexpr int XOR1 = 0xB1, XOR2 = 0x4E, XOR7 = 0x141, XOR8 = 0x128, NEWBC = 0x150;
__device__ __forceinline__ float tr16(float u, float v) {
  auto s = __builtin_amdgcn_permlane16_swap(asu(u), asu(v), false, false);
  return asf(s[0]) + asf(s[1]);
}
__device__ __forceinline__ float tr32(float u, float v) {
  auto s = __builtin_amdgcn_permlane32_swap(asu(u), asu(v), false, false);
  return asf(s[0]) + asf(s[1]);
}
__device__ __forceinline__ float mx16(float u) {
  auto s = __builtin_amdgcn_permlane16_swap(asu(u), asu(u), false, false);
  return fmaxf(asf(s[0]), asf(s[1]));
}
__device__ __forceinline__ float mx32(float u) {
  auto s = __builtin_amdgcn_permlane32_swap(asu(u), asu(u), false, false);
  return fmaxf(asf(s[0]), asf(s[1]));
}
template <int CTRL> __device__ __forceinline__ float trd(float lo, float hi, bool bit) {
  const float a = lo + dpp<CTRL>(lo), b = hi + dpp<CTRL>(hi);
  return bit ? b : a;
}
__device__ __forceinline__ float sum_lo3(float v) { v += dpp<XOR1>(v); v += dpp<XOR2>(v); v += dpp<XOR7>(v); return v; }
__device__ __forceinline__ float max_lo3(float v) { v = fmaxf(v, dpp<XOR1>(v)); v = fmaxf(v, dpp<XOR2>(v)); v = fmaxf(v, dpp<XOR7>(v)); return v; }
__device__ __forceinline__ float sum_hi3(float v) { v += dpp<XOR8>(v); v = tr16(v, v); v = tr32(v, v); return v; }
__device__ __forceinline__ void sum_hi3_2(float& a, float& b) {
  float t = tr32(a, b);
  t += dpp<XOR8>(t);
  t = tr16(t, t);
  auto s = __builtin_amdgcn_permlane32_swap(asu(t), asu(t), false, false);
  a = asf(s[0]); b = asf(s[1]);
}
__device__ __forceinline__ float wave_sum(float v) { return sum_hi3(sum_lo3(v)); }
__device__ __forceinline__ float wave_max(float v) { v = max_lo3(v); v = fmaxf(v, dpp<XOR8>(v)); return mx32(mx16(v)); }
constexpr float LOG2E = 1.4426950408889634f;
__device__ __forceinline__ float silu_f(float v) { return v * __builtin_amdgcn_rcpf(1.0f + __builtin_amdgcn_exp2f(-LOG2E * v)); }
__device__ __forceinline__ void wg_barrier() {
  asm volatile("s_waitcnt lgkmcnt(0)" ::: "memory");
  __builtin_amdgcn_s_barrier();
  asm volatile("" ::: "memory");
}
#define SB() __builtin_amdgcn_sched_barrier(0)
#define VMWAIT(n) asm volatile("s_waitcnt vmcnt(%0)" ::"i"(n) : "memory")
#define PIN(x) asm volatile("" : "+v"(x))
#define KEEP(x) asm volatile("" ::"v"(x))
constexpr int AUX_NT = 2, AUX_SC1 = 16;
typedef __amdgpu_buffer_rsrc_t rsrc_t;
__device__ __forceinline__ rsrc_t mk_rsrc(const char* p, unsigned bytes) { return __builtin_amdgcn_make_buffer_rsrc((void*)p, 0, (int)bytes, 0x00020000); }
template <int AUX> __device__ __forceinline__ v4u ld16(rsrc_t r, unsigned voff, unsigned soff) { return __builtin_amdgcn_raw_buffer_load_b128(r, voff, soff, AUX); }
template <int AUX> __device__ __forceinline__ unsigned ld4(rsrc_t r, unsigned voff) { return __builtin_amdgcn_raw_buffer_load_b32(r, voff, 0, AUX); }
__device__ __forceinline__ void put_cell(rsrc_t r, unsigned off, float a, float b, unsigned tag) {
  const v4u g = {asu(a), tag, asu(b), tag};
  __builtin_amdgcn_raw_buffer_store_b128(g, r, off, 0, AUX_SC1);
}
__device__ __forceinline__ bool any_lane(bool p) { return __builtin_amdgcn_ballot_w64(p) != 0; }
__device__ __forceinline__ bool cell_ok(const v4u& t, unsigned tag) { return ((t[1] ^ tag) | (t[3] ^ tag)) == 0u; }
template <int N, int SLP> __device__ __forceinline__ void get_cells(rsrc_t r, const unsigned (&off)[N], unsigned tag, v4u (&g)[N]) {
  unsigned o[N];
#pragma unroll
  for (int i = 0; i < N; ++i) o[i] = off[i];
#pragma nounroll
  for (unsigned spins = 0;; ++spins) {
    asm volatile("" ::: "memory");
    v4u t[N];
#pragma unroll
    for (int i = 0; i < N; ++i) t[i] = ld16<AUX_SC1>(r, o[i], 0);
    unsigned pend = OOB;
#pragma unroll
    for (int i = 0; i < N; ++i) {
      const bool hit = cell_ok(t[i], tag);
      g[i][0] = hit ? t[i][0] : g[i][0];
      g[i][2] = hit ? t[i][2] : g[i][2];
      o[i] = hit ? OOB : o[i];
      pend &= o[i];
    }
    if (__builtin_expect(!any_lane(pend != OOB), 1)) break;
    if (spins > SPIN_CAP) {
#pragma unroll
      for (int i = 0; i < N; ++i) g[i][0] = g[i][2] = 0x7fc00000u;
      break;
    }
    __builtin_amdgcn_s_sleep(SLP);
  }
}
__device__ __forceinline__ unsigned lds_get(LAS unsigned* p) {
  return __builtin_amdgcn_readfirstlane(__hip_atomic_load(p, __ATOMIC_RELAXED, __HIP_MEMORY_SCOPE_WORKGROUP));
}
__device__ __forceinline__ void lds_wait(LAS unsigned* p, unsigned want) {
#pragma nounroll
  for (unsigned spins = 0; lds_get(p) < want && spins < (SPIN_CAP << 6); ++spins) __builtin_amdgcn_s_sleep(1);
  asm volatile("" ::: "memory");
}
__device__ __forceinline__ void lds_post(LAS unsigned* p) {
  asm volatile("s_waitcnt lgkmcnt(0)" ::: "memory");
  if ((threadIdx.x & 63) == 0) __hip_atomic_fetch_add(p, 1u, __ATOMIC_RELAXED, __HIP_MEMORY_SCOPE_WORKGROUP);
}
__device__ __forceinline__ unsigned now() { return (unsigned)__builtin_amdgcn_s_memrealtime(); }
__device__ __forceinline__ void wait_until(unsigned t) {
#pragma nounroll
  while ((int)(now() - t) < 0) __builtin_amdgcn_s_sleep(1);
}
__device__ __forceinline__ unsigned xcc_id() { return (unsigned)__builtin_amdgcn_s_getreg(20 | (3 << 11)) & 0xfu; }
__device__ __forceinline__ void row_fma(float (&acc)[8], v4u w, unsigned xlo, unsigned xhi) {
#pragma unroll
  for (int k = 0; k < 4; ++k) {
    acc[2 * k] = dot2(w[k], xlo, acc[2 * k]);
    acc[2 * k + 1] = dot2(w[k], xhi, acc[2 * k + 1]);
  }
}
__device__ __forceinline__ float tr_lo3(const float (&a)[8], int lane) {
  float y[4], z[2];
#pragma unroll
  for (int k = 0; k < 4; ++k) y[k] = trd<XOR7>(a[k], a[k + 4], lane & 4);
#pragma unroll
  for (int k = 0; k < 2; ++k) z[k] = trd<XOR2>(y[k], y[k + 2], lane & 2);
  return trd<XOR1>(z[0], z[1], lane & 1);
}
__device__ __forceinline__ float tr_hi3(const float (&a)[8], int lane) {
  float y[4], z[2];
#pragma unroll
  for (int k = 0; k < 4; ++k) y[k] = tr32(a[k], a[k + 4]);
#pragma unroll
  for (int k = 0; k < 2; ++k) z[k] = tr16(y[k], y[k + 2]);
  return trd<XOR8>(z[0], z[1], lane & 8);
}
constexpr int brev4(int i) { return ((i & 1) << 3) | ((i & 2) << 1) | ((i & 4) >> 1) | ((i & 8) >> 3); }

struct Strm {
  v4u buf[2 * NL], wor[2], q;
  float l0, l1, l2, l3, sc[2], p[2], m, fw, fv, xr;
  v2f acc[4];
  unsigned tag, pre, tb_end;
  unsigned a_red_w, a_red_r, a_tp_w, a_tp_r, o_h1_w, o_h1_r, o_h2_w, o_h2_r;
  bool fin;
};
struct Ctx { rsrc_t rs, rk, rv, ro; unsigned voff, oo, toff; LAS unsigned* ldsu; int lane, c, sw, h, s, idx; };

template <int I> __device__ __forceinline__ void k_dot(Strm& z, int c) {
  const v4u k = z.buf[I];
  float d = dot2(k[0], z.q[0], 0.f);
  d = dot2(k[1], z.q[1], d);
  d = dot2(k[2], z.q[2], d);
  d = dot2(k[3], z.q[3], d);
  if constexpr ((I & 1) == 0) { z.l0 = d; return; }
  d = trd<XOR8>(z.l0, d, c & 8);
  if constexpr ((I & 2) == 0) { z.l1 = d; return; }
  d = trd<XOR7>(z.l1, d, c & 4);
  if constexpr ((I & 4) == 0) { z.l2 = d; return; }
  d = trd<XOR2>(z.l2, d, c & 2);
  if constexpr ((I & 8) == 0) { z.l3 = d; return; }
  z.sc[I >> 4] = trd<XOR1>(z.l3, d, c & 1);
}
template <int I> __device__ __forceinline__ void pv_step(Strm& z) {
  const v4u v = z.buf[NL + I];
  const float pb = dpp<NEWBC + brev4(I & 15)>(z.p[I >> 4]);
  const v2f pp = {pb, pb};
#pragma unroll
  for (int k = 0; k < 4; ++k) { const v2f vf = {lo_f(v[k]), hi_f(v[k])}; z.acc[k] = __builtin_elementwise_fma(pp, vf, z.acc[k]); }
  asm volatile("" : "+v"(z.acc[0]), "+v"(z.acc[1]), "+v"(z.acc[2]), "+v"(z.acc[3]));
}
constexpr int cmin(int a, int b) { return a < b ? a : b; }
constexpr int N_SM = NL - 1 + WIN, N_END = 2 * NL + 4;
constexpr int dots_done(int n) { return n < QS ? 0 : cmin(cmin(n - WIN + 1, NL), (n - QS + 1) * DPS); }
constexpr int pv_done(int n) { return n < N_SM + 2 ? 0 : n >= 2 * NL - 1 ? n - NL - 3 : cmin(n - NL - WIN + 1, 2 * (n - N_SM - 1)); }
static_assert(dots_done(N_SM) == NL && pv_done(N_END - 1) == NL && pv_done(2 * NL - 1) >= pv_done(2 * NL - 2), "slot plan");
static_assert(N_SM + 6 < PRET && PRET + WIN < 2 * NL && N_SM + 1 < X2SLOT && X2SLOT < 2 * NL - 1, "slot plan");
template <int LO, int HI> __device__ __forceinline__ void k_dots(Strm& z, int c) {
  if constexpr (LO < HI) { k_dot<LO>(z, c); k_dots<LO + 1, HI>(z, c); }
}
template <int LO, int HI> __device__ __forceinline__ void pv_steps(Strm& z) {
  if constexpr (LO < HI) { pv_step<LO>(z); pv_steps<LO + 1, HI>(z); }
}
template <int N, int END> __device__ __forceinline__ void slots(Strm& z, const Ctx& x) {
  if constexpr (N < END) {
    LAS float* ldsf = (LAS float*)x.ldsu;
    if constexpr (N < 2 * NL) {
      VMWAIT((N < PSTART ? WIN0 : WIN) - 1);
      if constexpr (N >= PSTART && N <= QS) wait_until(lds_get(x.ldsu + F_TB) + x.toff + ((N * PD) >> 4));
      if constexpr (N > QS) wait_until(z.tb_end + x.toff - (PA + 4 * PD - ((N * PD) >> 4)));
      z.buf[N] = ld16<AUX_NT>(N < NL ? x.rk : x.rv, x.voff, (N % NL) * 8192);
      SB();
    }
    if constexpr (N == QS) {
      lds_wait(x.ldsu + F_Q, 1);
      z.q = *(LAS v4u*)(x.ldsu + L_Q + 4 * x.c);
      z.tag = lds_get(x.ldsu + F_TAG);
      z.tb_end = lds_get(x.ldsu + F_TB) + PA + 4 * PD;
    }
    k_dots<dots_done(N - 1), dots_done(N)>(z, x.c);
    if constexpr (dots_done(N) > dots_done(N - 1)) asm volatile("" : "+v"(z.l0), "+v"(z.l1), "+v"(z.l2), "+v"(z.l3), "+v"(z.sc[0]), "+v"(z.sc[1]));
    if constexpr (N == N_SM) {
      constexpr float SCALE = 0.08838834764831845f;
      const float s0 = rbf(rbf(z.sc[0]) * SCALE), s1 = rbf(rbf(z.sc[1]) * SCALE);
      z.m = wave_max(fmaxf(s0, s1));
      z.p[0] = __builtin_amdgcn_exp2f((s0 - z.m) * LOG2E);
      z.p[1] = __builtin_amdgcn_exp2f((s1 - z.m) * LOG2E);
      asm volatile("" : "+v"(z.p[0]), "+v"(z.p[1]), "+v"(z.m));
    }
    if constexpr (N == N_SM + 1) {
      const v2f ml = {z.m, wave_sum(z.p[0] + z.p[1])};
      if (x.lane == 0) *(LAS v2f*)(ldsf + L_ML + 2 * x.sw) = ml;
      lds_post(x.ldsu + F_ML);
    }
    if constexpr (N == N_SM + 6) {
      const int lane = x.lane, hi3 = lane >> 3, lo3 = lane & 7, sw = x.sw;
      z.a_red_w = (L_RED + sw * 128 + 8 * x.c + 2 * (lane >> 4)) * 4;
      z.a_red_r = (L_RED + hi3 * 128 + 16 * sw + 2 * lo3) * 4;
      z.a_tp_w = (L_TP + sw * 64 + lane) * 4;
      z.a_tp_r = (L_TP + hi3 * 64 + 8 * sw + lo3) * 4;
      z.o_h1_w = lane < 8 ? WS_H1 + (x.h * NSPLIT + x.s) * H1_REC + sw * 128 + lane * 16 : OOB;
      z.o_h1_r = hi3 != x.s ? WS_H1 + (x.h * NSPLIT + hi3) * H1_REC + sw * 128 + lo3 * 16 : OOB;
      z.o_h2_w = lane < 8 ? WS_H2 + (x.s * NH + x.h) * H2_REC + sw * 128 + lane * 16 : OOB;
      const int job = 2 * x.idx + (sw & 1);
      z.o_h2_r = WS_H2 + ((job >> 3) * NH + hi3) * H2_REC + (job & 7) * 128 + lo3 * 16;
      z.xr = lo_f(((LAS unsigned short*)(x.ldsu + L_XR))[8 * (sw & 1) + lo3]);
      z.fin = sw < 2 && lds_get(x.ldsu + F_FIN) != 0u;
      asm volatile("" : "+v"(z.a_red_w), "+v"(z.a_red_r), "+v"(z.a_tp_w), "+v"(z.a_tp_r), "+v"(z.o_h1_w), "+v"(z.o_h1_r), "+v"(z.o_h2_w), "+v"(z.o_h2_r), "+v"(z.xr));
    }
    if constexpr (N == PRET)
      z.pre = ld4<AUX_SC1>(x.rs, x.lane == 0 ? z.o_h1_w : x.lane == 1 ? z.o_h2_w : OOB);
    if constexpr (N == PRET + WIN) KEEP(z.pre);
    if constexpr (N == N_END - 1) {
      VMWAIT(0);
      z.wor[0] = ld16<AUX_NT>(x.ro, x.oo, 0);
      z.wor[1] = ld16<AUX_NT>(x.ro, x.oo, D * 2);
      SB();
    }
    pv_steps<pv_done(N - 1), pv_done(N)>(z);
    if constexpr (N == X2SLOT) { if (x.sw == 7) lds_post(x.ldsu + F_PROG); }
    if constexpr (N == 2 * NL - 1) {
      lds_wait(x.ldsu + F_F, 1);
      z.fw = ldsf[L_F + x.sw];
      z.fv = ldsf[L_FV + 8 * x.sw + (x.lane & 7)];
      asm volatile("" : "+v"(z.fw), "+v"(z.fv));
    }
    SB();
    slots<N + 1, END>(z, x);
  }
}

__global__ void __launch_bounds__(NTHR, 3)
fused_decode(const char* pk, const char* px, char* pws, const char* pwq, const char* pw1, const char* pw2, const char* pv, const char* pwo, char* pout) {
  __shared__ __attribute__((aligned(16))) unsigned lds[L_END];
  LAS unsigned* ldsu = (LAS unsigned*)lds;
  LAS float* ldsf = (LAS float*)lds;
  LAS char* ldsb = (LAS char*)lds;
  const int tid = threadIdx.x, lane = tid & 63;
  const int w = __builtin_amdgcn_readfirstlane(tid >> 6);
  const int b = blockIdx.x;
  const char* pc0;
  unsigned code_bytes;
  asm volatile("s_getpc_b64 %0\n.Lfd_code_begin:\n\ts_mov_b32 %1, .Lfd_code_end-.Lfd_code_begin" : "=s"(pc0), "=s"(code_bytes));
  const unsigned warm0 = ld4<0>(mk_rsrc(pc0, code_bytes), tid * 64);
  const int h = (b & 7) * 4 + (b >> 6), s = (b >> 3) & 7;
  const int r = lane >> 4, c = lane & 15, hi3 = lane >> 3, lo3 = lane & 7;
  unsigned kvoff = (h * SK + s * 1024) * DH * 2, voff = (w - 1) * 1024 + lane * 16;
  asm volatile("" : "+s"(kvoff), "+v"(voff));

  if (w == 0) {
    __builtin_amdgcn_s_setprio(3);
    const rsrc_t rs = mk_rsrc(pws, WS_END);
    const int g0 = 5 * h + (h < 12 ? h : 12), ng = h < 12 ? 6 : 5;
    asm volatile("" ::"s"(pws));
    const unsigned tstart = now();
    v4u xv = ld16<0>(mk_rsrc(px, D * 2), lane * 16, 0);
    const unsigned epoch = ld4<AUX_SC1>(rs, WS_EPOCH);
    SB();
    v4u wq[16], w1r[8], w2r[6];
    {
      const rsrc_t rq = mk_rsrc(pwq, D * NH * DH * 2), r1 = mk_rsrc(pw1, D * FF * 2), r2 = mk_rsrc(pw2, FF * D * 2);
      const unsigned oq = ((64 * s + 16 * r) * (NH * DH) + h * DH + 8 * c) * 2;
#pragma unroll
      for (int i = 0; i < 16; ++i) wq[i] = ld16<AUX_NT>(rq, oq, i * (NH * DH * 2));
      SB();
      const unsigned o1 = ((64 * s + 8 * hi3) * FF + 8 * (g0 + (lo3 < ng ? lo3 : ng - 1))) * 2;
#pragma unroll
      for (int i = 0; i < 8; ++i) w1r[i] = ld16<AUX_NT>(r1, o1, i * (FF * 2));
      const unsigned o2 = ((8 * g0 + hi3) * D + 64 * s + 8 * lo3) * 2;
#pragma unroll
      for (int j = 0; j < 6; ++j) w2r[j] = ld16<AUX_NT>(r2, o2, (j < ng ? j : ng - 1) * (8 * D * 2));
    }
    if (lane < 8) ldsu[L_FLAG + lane] = L_FLAG + lane == F_TB ? tstart : 0u;
    SB();
    wg_barrier();
    SB();
    PIN(xv);
    float xf[8];
#pragma unroll
    for (int k = 0; k < 4; ++k) { xf[2 * k] = lo_f(xv[k]); xf[2 * k + 1] = hi_f(xv[k]); }
    float sm = 0.f;
#pragma unroll
    for (int k = 0; k < 8; ++k) sm += xf[k];
    const float mean = wave_sum(sm) * (1.0f / D);
    float sq = 0.f;
#pragma unroll
    for (int k = 0; k < 8; ++k) { const float d = xf[k] - mean; sq = fmaf(d, d, sq); }
    const float var = wave_sum(sq) * (1.0f / D);
    const float mb = rbf(mean), den = rbf(sqrtf(rbf(rbf(var) + 1e-5f)));
    v4u xn;
#pragma unroll
    for (int k = 0; k < 4; ++k) xn[k] = pk_bf16(rbf(xf[2 * k] - mb) / den, rbf(xf[2 * k + 1] - mb) / den);
    if (hi3 == s) *(LAS v4u*)(ldsu + L_XS + lo3 * 4) = xn;
    if ((lane >> 1) == (b >> 3)) *(LAS v4u*)(ldsu + L_XR + (lane & 1) * 4) = xv;
    asm volatile("s_waitcnt lgkmcnt(0)" ::: "memory");
    const v4u xa = *(LAS v4u*)(ldsu + L_XS + r * 8), xb = *(LAS v4u*)(ldsu + L_XS + r * 8 + 4), xc = *(LAS v4u*)(ldsu + L_XS + hi3 * 4);
    unsigned tag = __builtin_amdgcn_readfirstlane(epoch) * 1664525u + 1013904223u;
    if (tag == 0u) tag = 1013904223u;
    if (lane == 0) ldsu[F_TAG] = tag;
    put_cell(rs, b < 8 && lane < 4 ? WS_T + b * 128 + lane * 16 : OOB, asf(tstart), asf(xcc_id()), tag);
    const unsigned x1_me = WS_X1 + (h * NSPLIT + s) * X1_REC, hcell = 1024 + (r * 8 + lo3) * 16;
    {
      float acc[8], hcc[8], y[4];
#pragma unroll
      for (int k = 0; k < 8; ++k) acc[k] = hcc[k] = 0.f;
#pragma unroll
      for (int i = 0; i < 16; i += 2) {
        const unsigned P = i < 8 ? xa[i >> 1] : xb[(i - 8) >> 1];
        row_fma(acc, wq[i], P & 0xffffu, P << 16);
        row_fma(acc, wq[i + 1], P >> 16, P & 0xffff0000u);
      }
#pragma unroll
      for (int k = 0; k < 4; ++k) y[k] = tr32(acc[k], acc[k + 4]);
      put_cell(rs, x1_me + lane * 16, tr16(y[0], y[2]), tr16(y[1], y[3]), tag);
#pragma unroll
      for (int i = 0; i < 8; i += 2) {
        const unsigned P = xc[i >> 1];
        row_fma(hcc, w1r[i], P & 0xffffu, P << 16);
        row_fma(hcc, w1r[i + 1], P >> 16, P & 0xffff0000u);
      }
#pragma unroll
      for (int k = 0; k < 4; ++k) y[k] = tr32(hcc[k], hcc[k + 4]);
      float z0 = tr16(y[0], y[2]), z1 = tr16(y[1], y[3]);
      z0 += dpp<XOR8>(z0); z1 += dpp<XOR8>(z1);
      put_cell(rs, (lane & 8) == 0 ? x1_me + hcell : OOB, z0, z1, tag);
    }
    const unsigned warm2 = ld4<AUX_NT>(mk_rsrc(pv + kvoff, 1024 * DH * 2), lane * 65536);
    const unsigned warm3 = ld4<AUX_NT>(mk_rsrc(pwo + h * (DH * D * 2), DH * D * 2), lane * 65536);
    unsigned warm1;
    {
      v4u g[13];
      unsigned off[13];
      const unsigned x1_h = WS_X1 + h * NSPLIT * X1_REC;
#pragma unroll
      for (int i = 0; i < 8; ++i) off[i] = x1_h + lane * 16 + i * X1_REC;
#pragma unroll
      for (int i = 0; i < 4; ++i) off[8 + i] = x1_h + (2 * i + (hi3 & 1)) * X1_REC + hcell;
      off[12] = WS_T + lo3 * 128;
#pragma unroll
      for (int i = 0; i < 13; ++i) g[i] = v4u{0u, 0u, 0u, 0u};
      __builtin_amdgcn_s_sleep(X1DELAY);
      get_cells<13, 8>(rs, off, tag, g);
      KEEP(warm0); KEEP(warm2); KEEP(warm3);
      float q0 = 0.f, q1 = 0.f;
#pragma unroll
      for (int i = 0; i < 8; ++i) { q0 += asf(g[i][0]); q1 += asf(g[i][2]); }
      ldsu[L_Q + 4 * c + r] = pk_bf16(q0, q1);
      const float db = sum_lo3((float)(int)(g[12][0] - tstart)) * 0.125f;
      const unsigned onx = (unsigned)__builtin_amdgcn_ballot_w64(g[12][2] == FINX) & 0xffu;
      const bool fin = (onx ? __builtin_ctz(onx) : 0) == (b & 7);
      if (lane == 0) { ldsu[F_TB] = tstart + (int)fminf(fmaxf(db, -300.f), 300.f); ldsu[F_FIN] = fin; }
      lds_post(ldsu + F_Q);
      warm1 = ld4<0>(mk_rsrc(pout, fin ? D * 2 : 0), (b >> 3) * 32);
      const float h0 = (asf(g[8][0]) + asf(g[9][0])) + (asf(g[10][0]) + asf(g[11][0]));
      const float h1 = (asf(g[8][2]) + asf(g[9][2])) + (asf(g[10][2]) + asf(g[11][2]));
      const float hp = rbf(trd<XOR8>(h0, h1, lane & 8));
      const float hs = lo3 < ng ? rbf(hp / (1.0f + __expf(-hp))) : 0.f;
      float fa[8];
#pragma unroll
      for (int k = 0; k < 8; ++k) fa[k] = 0.f;
#pragma unroll
      for (int j = 0; j < 6; ++j) {
        const unsigned hj = __builtin_amdgcn_ds_bpermute(((lane & 56) | j) * 4, asu(hs));
        row_fma(fa, w2r[j], hj >> 16, hj);
      }
      ldsf[L_FV + 8 * lo3 + hi3] = tr_hi3(fa, lane);
    }
    lds_wait(ldsu + F_ML, NSW);
    {
      const v2f ml = *(LAS v2f*)(ldsf + L_ML + 2 * lo3);
      const float M = max_lo3(ml[0]);
      const float L = sum_lo3(ml[1] * __builtin_amdgcn_exp2f((ml[0] - M) * LOG2E));
      put_cell(rs, lane < 4 ? WS_X2 + (h * NSPLIT + s) * X2_REC + lane * 16 : OOB, M, L, tag);
      lds_wait(ldsu + F_PROG, 1);
      v4u g[1] = {{0u, 0u, 0u, 0u}};
      const unsigned off[1] = {WS_X2 + (h * NSPLIT + lo3) * X2_REC};
      get_cells<1, 8>(rs, off, tag, g);
      KEEP(warm1);
      const float Mh = max_lo3(asf(g[0][0]));
      const float Lh = sum_lo3(asf(g[0][2]) * __builtin_amdgcn_exp2f((asf(g[0][0]) - Mh) * LOG2E));
      const float f = __builtin_amdgcn_exp2f((ml[0] - Mh) * LOG2E) / Lh;
      if (lane < 8) ldsf[L_F + lane] = f;
      lds_post(ldsu + F_F);
    }
    asm volatile(".Lfd_code_end:");
    return;
  }

  const int sw = w - 1;
  Ctx x;
  x.rk = mk_rsrc(pk + kvoff, 1024 * DH * 2);
  x.voff = voff;
  Strm z;
#pragma unroll
  for (int i = 0; i < WIN0; ++i) z.buf[i] = ld16<AUX_NT>(x.rk, x.voff, i * 8192);
  SB();
  const rsrc_t rs = mk_rsrc(pws, WS_END);
  x.rs = rs;
  x.rv = mk_rsrc(pv + kvoff, 1024 * DH * 2);
  x.ro = mk_rsrc(pwo, NH * DH * D * 2);
  x.oo = ((h * DH + 16 * sw + 2 * hi3) * D + 64 * s + 8 * lo3) * 2;
  x.toff = PA + ((sw * PD) >> 7);
  x.ldsu = ldsu; x.lane = lane; x.c = c; x.sw = sw; x.h = h; x.s = s; x.idx = b >> 3;
  z.l0 = z.l1 = z.l2 = z.l3 = z.sc[0] = z.sc[1] = 0.f;
#pragma unroll
  for (int k = 0; k < 4; ++k) z.acc[k] = v2f{0.f, 0.f};
  wg_barrier();
  slots<WIN0, 8>(z, x);
  KEEP(warm0);
  slots<8, N_END>(z, x);
  const unsigned tag = z.tag;
  {
    const float y0 = tr32(z.acc[0][0], z.acc[2][0]), y1 = tr32(z.acc[0][1], z.acc[2][1]), y2 = tr32(z.acc[1][0], z.acc[3][0]), y3 = tr32(z.acc[1][1], z.acc[3][1]);
    const v2f o = {tr16(y0, y2) * z.fw, tr16(y1, y3) * z.fw};
    *(LAS v2f*)(ldsb + z.a_red_w) = o;
  }
  wg_barrier();
  {
    const v2f o = *(const LAS v2f*)(ldsb + z.a_red_r);
    float o0 = o[0], o1 = o[1];
    sum_hi3_2(o0, o1);
    put_cell(rs, z.o_h1_w, o0, o1, tag);
    SB();
    float ta[8];
#pragma unroll
    for (int k = 0; k < 8; ++k) { ta[k] = 0.f; PIN(ta[k]); }
    __builtin_amdgcn_s_sleep(D1);
    wait_until(z.tb_end + C1);
    asm volatile("" ::: "memory");
    const v4u t1 = ld16<AUX_SC1>(rs, z.o_h1_r, 0);
    SB();
    unsigned wp[8];
#pragma unroll
    for (int k = 0; k < 4; ++k) {
      wp[2 * k] = __builtin_amdgcn_perm(z.wor[1][k], z.wor[0][k], 0x05040100u);
      wp[2 * k + 1] = __builtin_amdgcn_perm(z.wor[1][k], z.wor[0][k], 0x07060302u);
    }
#pragma unroll
    for (int k = 0; k < 8; ++k) wp[k] = __builtin_amdgcn_ds_bpermute((lo3 * 8 + hi3) * 4, wp[k]);
#pragma unroll
    for (int k = 0; k < 8; ++k) PIN(wp[k]);
    SB();
    const bool hit = cell_ok(t1, tag);
    v4u g[1] = {{hit ? t1[0] : asu(o0), 0u, hit ? t1[2] : asu(o1), 0u}};
    const unsigned off[1] = {hit ? OOB : z.o_h1_r};
    if (__builtin_expect(any_lane(off[0] != OOB), 0)) get_cells<1, 2>(rs, off, tag, g);
    float a0 = asf(g[0][0]), a1 = asf(g[0][2]);
    sum_hi3_2(a0, a1);
    const unsigned ap = pk_bf16(a0, a1);
#pragma unroll
    for (int k = 0; k < 8; ++k) ta[k] = dot2(wp[k], ap, ta[k]);
    *(LAS float*)(ldsb + z.a_tp_w) = tr_lo3(ta, lane);
    wg_barrier();
    put_cell(rs, z.o_h2_w, sum_hi3(*(const LAS float*)(ldsb + z.a_tp_r)), z.fv, tag);
  }
  if (z.fin) {
    v4u g[4];
    unsigned off[4];
#pragma unroll
    for (int i = 0; i < 4; ++i) { g[i] = v4u{0u, 0u, 0u, 0u}; off[i] = z.o_h2_r + i * 8 * H2_REC; }
    const rsrc_t rout = mk_rsrc(pout, D * 2);
    unsigned ooff = lane < 8 ? (16 * (b >> 3) + 8 * sw + lane) * 2 : OOB;
    PIN(ooff);
    __builtin_amdgcn_s_sleep(D2);
    get_cells<4, 2>(rs, off, tag, g);
    float st = (asf(g[0][0]) + asf(g[1][0])) + (asf(g[2][0]) + asf(g[3][0]));
    float sf = (asf(g[0][2]) + asf(g[1][2])) + (asf(g[2][2]) + asf(g[3][2]));
    sum_hi3_2(st, sf);
    const float o = z.xr + rbf(rbf(st) * rbf(silu_f(rbf(sf))));
    __builtin_amdgcn_raw_buffer_store_b16((unsigned short)(pk_bf16(o, o) & 0xffffu), rout, ooff, 0, 0);
    if (b < 8 && sw == 0) put_cell(rs, lane < 8 ? WS_EPOCH + lane * 16 : OOB, asf(tag), asf(tag), tag);
  }
}

}

extern "C" void kernel_launch(void* const* d_in, const int* in_sizes, int n_in, void* d_out, int out_size, void* d_ws,
                              size_t ws_size, hipStream_t stream) {
  (void)in_sizes; (void)n_in; (void)out_size; (void)ws_size;
  fused_decode<<<dim3(NH * NSPLIT), dim3(NTHR), 0, stream>>>(
      (const char*)d_in[1], (const char*)d_in[0], (char*)d_ws, (const char*)d_in[3], (const char*)d_in[6], (const char*)d_in[7],
      (const char*)d_in[2], (const char*)d_in[4], (char*)d_out);
}
```

```cpp
#include <hip/hip_runtime.h>
#include <stdint.h>

#define GAS __attribute__((address_space(1)))
#define LAS __attribute__((address_space(3)))
typedef unsigned v4u __attribute__((ext_vector_type(4)));
typedef float v2f __attribute__((ext_vector_type(2)));
typedef __bf16 bf16x2 __attribute__((ext_vector_type(2)));

namespace {

constexpr int WIN = 6, WIN0 = 3, PSTART = 10;
constexpr int PA = 50, PD = 448;
constexpr int CKA = 20, CKS = 100, CKR = 170, CKM = 5, TBL = 36;
constexpr int QS = 26, DPS = 5;
constexpr int PRET = 46, X2SLOT = 50;
constexpr int X1DELAY = 32, C1 = 120, D1 = 4, D2 = 6;
constexpr unsigned FINX = 0;

constexpr int D = 512, NH = 32, SK = 8192, DH = 128, FF = 1376;
constexpr int NSPLIT = 8, NSW = 8, NTHR = 64 * (NSW + 1);
constexpr int NL = SK / NSPLIT / NSW / 4;

constexpr unsigned WS_EPOCH = 0;
constexpr unsigned WS_T = 1024;
constexpr unsigned WS_ACC = 2048;
constexpr unsigned WS_X1 = 4096, X1_REC = 1024;
constexpr unsigned WS_X2 = WS_X1 + 256 * X1_REC, X2_REC = 128;
constexpr unsigned WS_H1 = WS_X2 + 256 * X2_REC, H1_REC = 1024;
constexpr unsigned WS_H2 = WS_H1 + 256 * H1_REC, H2_REC = 1024;
constexpr unsigned WS_END = WS_H2 + 256 * H2_REC;
constexpr unsigned SPIN_CAP = 1u << 16, OOB = 0x7fffffffu;

constexpr int L_RED = 0;
constexpr int L_TP = L_RED + NSW * 128;
constexpr int L_FV = L_TP + NSW * 64;
constexpr int L_XS = L_FV + 64;
constexpr int L_XR = L_XS + 64;
constexpr int L_Q = L_XR + 8;
constexpr int L_ML = L_Q + 64;
constexpr int L_F = L_ML + 16;
constexpr int L_FLAG = L_F + 8;
constexpr int F_Q = L_FLAG, F_ML = L_FLAG + 1, F_F = L_FLAG + 2, F_TAG = L_FLAG + 3, F_PROG = L_FLAG + 4, F_TB = L_FLAG + 5, F_FIN = L_FLAG + 6;
constexpr int L_LT = L_FLAG + 8;
constexpr int L_END = L_LT + 8;

__device__ __forceinline__ float asf(unsigned u) { return __builtin_bit_cast(float, u); }
__device__ __forceinline__ unsigned asu(float f) { return __builtin_bit_cast(unsigned, f); }
__device__ __forceinline__ float lo_f(unsigned u) { return asf(u << 16); }
__device__ __forceinline__ float hi_f(unsigned u) { return asf(u & 0xffff0000u); }
__device__ __forceinline__ unsigned pk_bf16(float lo, float hi) {
  bf16x2 v = {(__bf16)lo, (__bf16)hi};
  return __builtin_bit_cast(unsigned, v);
}
__device__ __forceinline__ float rbf(float x) { return (float)(__bf16)x; }
__device__ __forceinline__ float dot2(unsigned a, unsigned b, float c) {
  return __builtin_amdgcn_fdot2_f32_bf16(__builtin_bit_cast(bf16x2, a), __builtin_bit_cast(bf16x2, b), c, false);
}
template <int CTRL> __device__ __forceinline__ float dpp(float x) {
  return asf(__builtin_amdgcn_mov_dpp(__builtin_bit_cast(int, x), CTRL, 0xf, 0xf, true));
}
constexpr int XOR1 = 0xB1, XOR2 = 0x4E, XOR7 = 0x141, XOR8 = 0x128, NEWBC = 0x150;
__device__ __forceinline__ float tr16(float u, float v) {
  auto s = __builtin_amdgcn_permlane16_swap(asu(u), asu(v), false, false);
  return asf(s[0]) + asf(s[1]);
}
__device__ __forceinline__ float tr32(float u, float v) {
  auto s = __builtin_amdgcn_permlane32_swap(asu(u), asu(v), false, false);
  return asf(s[0]) + asf(s[1]);
}
__device__ __forceinline__ float mx16(float u) {
  auto s = __builtin_amdgcn_permlane16_swap(asu(u), asu(u), false, false);
  return fmaxf(asf(s[0]), asf(s[1]));
}
__device__ __forceinline__ float mx32(float u) {
  auto s = __builtin_amdgcn_permlane32_swap(asu(u), asu(u), false, false);
  return fmaxf(asf(s[0]), asf(s[1]));
}
template <int CTRL> __device__ __forceinline__ float trd(float lo, float hi, bool bit) {
  const float a = lo + dpp<CTRL>(lo), b = hi + dpp<CTRL>(hi);
  return bit ? b : a;
}
__device__ __forceinline__ float sum_lo3(float v) { v += dpp<XOR1>(v); v += dpp<XOR2>(v); v += dpp<XOR7>(v); return v; }
__device__ __forceinline__ float max_lo3(float v) { v = fmaxf(v, dpp<XOR1>(v)); v = fmaxf(v, dpp<XOR2>(v)); v = fmaxf(v, dpp<XOR7>(v)); return v; }
__device__ __forceinline__ float sum_hi3(float v) { v += dpp<XOR8>(v); v = tr16(v, v); v = tr32(v, v); return v; }
__device__ __forceinline__ void sum_hi3_2(float& a, float& b) {
  float t = tr32(a, b);
  t += dpp<XOR8>(t);
  t = tr16(t, t);
  auto s = __builtin_amdgcn_permlane32_swap(asu(t), asu(t), false, false);
  a = asf(s[0]); b = asf(s[1]);
}
__device__ __forceinline__ float wave_sum(float v) { return sum_hi3(sum_lo3(v)); }
__device__ __forceinline__ float wave_max(float v) { v = max_lo3(v); v = fmaxf(v, dpp<XOR8>(v)); return mx32(mx16(v)); }
constexpr float LOG2E = 1.4426950408889634f;
__device__ __forceinline__ float silu_f(float v) { return v * __builtin_amdgcn_rcpf(1.0f + __builtin_amdgcn_exp2f(-LOG2E * v)); }
__device__ __forceinline__ void wg_barrier() {
  asm volatile("s_waitcnt lgkmcnt(0)" ::: "memory");
  __builtin_amdgcn_s_barrier();
  asm volatile("" ::: "memory");
}
#define SB() __builtin_amdgcn_sched_barrier(0)
#define VMWAIT(n) asm volatile("s_waitcnt vmcnt(%0)" ::"i"(n) : "memory")
#define PIN(x) asm volatile("" : "+v"(x))
#define KEEP(x) asm volatile("" ::"v"(x))
constexpr int AUX_NT = 2, AUX_SC1 = 16;
typedef __amdgpu_buffer_rsrc_t rsrc_t;
__device__ __forceinline__ rsrc_t mk_rsrc(const char* p, unsigned bytes) { return __builtin_amdgcn_make_buffer_rsrc((void*)p, 0, (int)bytes, 0x00020000); }
template <int AUX> __device__ __forceinline__ v4u ld16(rsrc_t r, unsigned voff, unsigned soff) { return __builtin_amdgcn_raw_buffer_load_b128(r, voff, soff, AUX); }
template <int AUX> __device__ __forceinline__ unsigned ld4(rsrc_t r, unsigned voff) { return __builtin_amdgcn_raw_buffer_load_b32(r, voff, 0, AUX); }
__device__ __forceinline__ void put_cell(rsrc_t r, unsigned off, float a, float b, unsigned tag) {
  const v4u g = {asu(a), tag, asu(b), tag};
  __builtin_amdgcn_raw_buffer_store_b128(g, r, off, 0, AUX_SC1);
}
__device__ __forceinline__ bool any_lane(bool p) { return __builtin_amdgcn_ballot_w64(p) != 0; }
__device__ __forceinline__ bool cell_ok(const v4u& t, unsigned tag) { return ((t[1] ^ tag) | (t[3] ^ tag)) == 0u; }
template <int N, int SLP> __device__ __forceinline__ void get_cells(rsrc_t r, const unsigned (&off)[N], unsigned tag, v4u (&g)[N]) {
  unsigned o[N];
#pragma unroll
  for (int i = 0; i < N; ++i) o[i] = off[i];
#pragma nounroll
  for (unsigned spins = 0;; ++spins) {
    asm volatile("" ::: "memory");
    v4u t[N];
#pragma unroll
    for (int i = 0; i < N; ++i) t[i] = ld16<AUX_SC1>(r, o[i], 0);
    unsigned pend = OOB;
#pragma unroll
    for (int i = 0; i < N; ++i) {
      const bool hit = cell_ok(t[i], tag);
      g[i][0] = hit ? t[i][0] : g[i][0];
      g[i][2] = hit ? t[i][2] : g[i][2];
      o[i] = hit ? OOB : o[i];
      pend &= o[i];
    }
    if (__builtin_expect(!any_lane(pend != OOB), 1)) break;
    if (spins > SPIN_CAP) {
#pragma unroll
      for (int i = 0; i < N; ++i) g[i][0] = g[i][2] = 0x7fc00000u;
      break;
    }
    __builtin_amdgcn_s_sleep(SLP);
  }
}
__device__ __forceinline__ unsigned lds_get(LAS unsigned* p) {
  return __builtin_amdgcn_readfirstlane(__hip_atomic_load(p, __ATOMIC_RELAXED, __HIP_MEMORY_SCOPE_WORKGROUP));
}
__device__ __forceinline__ void lds_wait(LAS unsigned* p, unsigned want) {
#pragma nounroll
  for (unsigned spins = 0; lds_get(p) < want && spins < (SPIN_CAP << 6); ++spins) __builtin_amdgcn_s_sleep(1);
  asm volatile("" ::: "memory");
}
__device__ __forceinline__ void lds_post(LAS unsigned* p) {
  asm volatile("s_waitcnt lgkmcnt(0)" ::: "memory");
  if ((threadIdx.x & 63) == 0) __hip_atomic_fetch_add(p, 1u, __ATOMIC_RELAXED, __HIP_MEMORY_SCOPE_WORKGROUP);
}
__device__ __forceinline__ unsigned now() { return (unsigned)__builtin_amdgcn_s_memrealtime(); }
__device__ __forceinline__ void wait_until(unsigned t) {
#pragma nounroll
  while ((int)(now() - t) < 0) __builtin_amdgcn_s_sleep(1);
}
__device__ __forceinline__ unsigned xcc_id() { return (unsigned)__builtin_amdgcn_s_getreg(20 | (3 << 11)) & 0xfu; }
__device__ __forceinline__ void row_fma(float (&acc)[8], v4u w, unsigned xlo, unsigned xhi) {
#pragma unroll
  for (int k = 0; k < 4; ++k) {
    acc[2 * k] = dot2(w[k], xlo, acc[2 * k]);
    acc[2 * k + 1] = dot2(w[k], xhi, acc[2 * k + 1]);
  }
}
__device__ __forceinline__ float tr_lo3(const float (&a)[8], int lane) {
  float y[4], z[2];
#pragma unroll
  for (int k = 0; k < 4; ++k) y[k] = trd<XOR7>(a[k], a[k + 4], lane & 4);
#pragma unroll
  for (int k = 0; k < 2; ++k) z[k] = trd<XOR2>(y[k], y[k + 2], lane & 2);
  return trd<XOR1>(z[0], z[1], lane & 1);
}
__device__ __forceinline__ float tr_hi3(const float (&a)[8], int lane) {
  float y[4], z[2];
#pragma unroll
  for (int k = 0; k < 4; ++k) y[k] = tr32(a[k], a[k + 4]);
#pragma unroll
  for (int k = 0; k < 2; ++k) z[k] = tr16(y[k], y[k + 2]);
  return trd<XOR8>(z[0], z[1], lane & 8);
}
constexpr int brev4(int i) { return ((i & 1) << 3) | ((i & 2) << 1) | ((i & 4) >> 1) | ((i & 8) >> 3); }

struct Strm {
  v4u buf[2 * NL], wor[2], q;
  float l0, l1, l2, l3, sc[2], p[2], m, fw, fv, xr;
  v2f acc[4];
  unsigned tag, pre, tb_end;
  unsigned a_red_w, a_red_r, a_tp_w, a_tp_r, o_h1_w, o_h1_r, o_h2_w, o_h2_r;
  bool fin;
};
struct Ctx { rsrc_t rs, rk, rv, ro; unsigned voff, oo, toff; LAS unsigned* ldsu; int lane, c, sw, h, s, idx; };

template <int I> __device__ __forceinline__ void k_dot(Strm& z, int c) {
  const v4u k = z.buf[I];
  float d = dot2(k[0], z.q[0], 0.f);
  d = dot2(k[1], z.q[1], d);
  d = dot2(k[2], z.q[2], d);
  d = dot2(k[3], z.q[3], d);
  if constexpr ((I & 1) == 0) { z.l0 = d; return; }
  d = trd<XOR8>(z.l0, d, c & 8);
  if constexpr ((I & 2) == 0) { z.l1 = d; return; }
  d = trd<XOR7>(z.l1, d, c & 4);
  if constexpr ((I & 4) == 0) { z.l2 = d; return; }
  d = trd<XOR2>(z.l2, d, c & 2);
  if constexpr ((I & 8) == 0) { z.l3 = d; return; }
  z.sc[I >> 4] = trd<XOR1>(z.l3, d, c & 1);
}
template <int I> __device__ __forceinline__ void pv_step(Strm& z) {
  const v4u v = z.buf[NL + I];
  const float pb = dpp<NEWBC + brev4(I & 15)>(z.p[I >> 4]);
  const v2f pp = {pb, pb};
#pragma unroll
  for (int k = 0; k < 4; ++k) { const v2f vf = {lo_f(v[k]), hi_f(v[k])}; z.acc[k] = __builtin_elementwise_fma(pp, vf, z.acc[k]); }
  asm volatile("" : "+v"(z.acc[0]), "+v"(z.acc[1]), "+v"(z.acc[2]), "+v"(z.acc[3]));
}
constexpr int cmin(int a, int b) { return a < b ? a : b; }
constexpr int N_SM = NL - 1 + WIN, N_END = 2 * NL + 4;
constexpr int dots_done(int n) { return n < QS ? 0 : cmin(cmin(n - WIN + 1, NL), (n - QS + 1) * DPS); }
constexpr int pv_done(int n) { return n < N_SM + 2 ? 0 : n >= 2 * NL - 1 ? n - NL - 3 : cmin(n - NL - WIN + 1, 2 * (n - N_SM - 1)); }
static_assert(dots_done(N_SM) == NL && pv_done(N_END - 1) == NL && pv_done(2 * NL - 1) >= pv_done(2 * NL - 2), "slot plan");
static_assert(N_SM + 6 < PRET && PRET + WIN < 2 * NL && N_SM + 1 < X2SLOT && X2SLOT < 2 * NL - 1, "slot plan");
template <int LO, int HI> __device__ __forceinline__ void k_dots(Strm& z, int c) {
  if constexpr (LO < HI) { k_dot<LO>(z, c); k_dots<LO + 1, HI>(z, c); }
}
template <int LO, int HI> __device__ __forceinline__ void pv_steps(Strm& z) {
  if constexpr (LO < HI) { pv_step<LO>(z); pv_steps<LO + 1, HI>(z); }
}
template <int N, int END> __device__ __forceinline__ void slots(Strm& z, const Ctx& x) {
  if constexpr (N < END) {
    LAS float* ldsf = (LAS float*)x.ldsu;
    if constexpr (N < 2 * NL) {
      VMWAIT(N == CKA ? 2 : (N < PSTART ? WIN0 : WIN) - 1);
      if constexpr (N == CKA) { const unsigned t = (now() - x.toff) | 1u; if (x.lane == 0) x.ldsu[L_LT + x.sw] = t; }
      if constexpr (N >= PSTART && N <= TBL) wait_until(lds_get(x.ldsu + F_TB) + x.toff + ((N * PD) >> 4));
      if constexpr (N > TBL) wait_until(z.tb_end + x.toff - (PA + 4 * PD - ((N * PD) >> 4)));
      z.buf[N] = ld16<AUX_NT>(N < NL ? x.rk : x.rv, x.voff, (N % NL) * 8192);
      SB();
    }
    if constexpr (N == QS) {
      lds_wait(x.ldsu + F_Q, 1);
      z.q = *(LAS v4u*)(x.ldsu + L_Q + 4 * x.c);
      z.tag = lds_get(x.ldsu + F_TAG);
    }
    if constexpr (N == TBL) z.tb_end = lds_get(x.ldsu + F_TB) + PA + 4 * PD;
    k_dots<dots_done(N - 1), dots_done(N)>(z, x.c);
    if constexpr (dots_done(N) > dots_done(N - 1)) asm volatile("" : "+v"(z.l0), "+v"(z.l1), "+v"(z.l2), "+v"(z.l3), "+v"(z.sc[0]), "+v"(z.sc[1]));
    if constexpr (N == N_SM) {
      constexpr float SCALE = 0.08838834764831845f;
      const float s0 = rbf(rbf(z.sc[0]) * SCALE), s1 = rbf(rbf(z.sc[1]) * SCALE);
      z.m = wave_max(fmaxf(s0, s1));
      z.p[0] = __builtin_amdgcn_exp2f((s0 - z.m) * LOG2E);
      z.p[1] = __builtin_amdgcn_exp2f((s1 - z.m) * LOG2E);
      asm volatile("" : "+v"(z.p[0]), "+v"(z.p[1]), "+v"(z.m));
    }
    if constexpr (N == N_SM + 1) {
      const v2f ml = {z.m, wave_sum(z.p[0] + z.p[1])};
      if (x.lane == 0) *(LAS v2f*)(ldsf + L_ML + 2 * x.sw) = ml;
      lds_post(x.ldsu + F_ML);
    }
    if constexpr (N == N_SM + 6) {
      const int lane = x.lane, hi3 = lane >> 3, lo3 = lane & 7, sw = x.sw;
      z.a_red_w = (L_RED + sw * 128 + 8 * x.c + 2 * (lane >> 4)) * 4;
      z.a_red_r = (L_RED + hi3 * 128 + 16 * sw + 2 * lo3) * 4;
      z.a_tp_w = (L_TP + sw * 64 + lane) * 4;
      z.a_tp_r = (L_TP + hi3 * 64 + 8 * sw + lo3) * 4;
      z.o_h1_w = lane < 8 ? WS_H1 + (x.h * NSPLIT + x.s) * H1_REC + sw * 128 + lane * 16 : OOB;
      z.o_h1_r = hi3 != x.s ? WS_H1 + (x.h * NSPLIT + hi3) * H1_REC + sw * 128 + lo3 * 16 : OOB;
      z.o_h2_w = lane < 8 ? WS_H2 + (x.s * NH + x.h) * H2_REC + sw * 128 + lane * 16 : OOB;
      const int job = 2 * x.idx + (sw & 1);
      z.o_h2_r = WS_H2 + ((job >> 3) * NH + hi3) * H2_REC + (job & 7) * 128 + lo3 * 16;
      z.xr = lo_f(((LAS unsigned short*)(x.ldsu + L_XR))[8 * (sw & 1) + lo3]);
      z.fin = sw < 2 && lds_get(x.ldsu + F_FIN) != 0u;
      asm volatile("" : "+v"(z.a_red_w), "+v"(z.a_red_r), "+v"(z.a_tp_w), "+v"(z.a_tp_r), "+v"(z.o_h1_w), "+v"(z.o_h1_r), "+v"(z.o_h2_w), "+v"(z.o_h2_r), "+v"(z.xr));
    }
    if constexpr (N == PRET)
      z.pre = ld4<AUX_SC1>(x.rs, x.lane == 0 ? z.o_h1_w : x.lane == 1 ? z.o_h2_w : OOB);
    if constexpr (N == PRET + WIN) KEEP(z.pre);
    if constexpr (N == N_END - 1) {
      VMWAIT(0);
      z.wor[0] = ld16<AUX_NT>(x.ro, x.oo, 0);
      z.wor[1] = ld16<AUX_NT>(x.ro, x.oo, D * 2);
      SB();
    }
    pv_steps<pv_done(N - 1), pv_done(N)>(z);
    if constexpr (N == X2SLOT) { if (x.sw == 7) lds_post(x.ldsu + F_PROG); }
    if constexpr (N == 2 * NL - 1) {
      lds_wait(x.ldsu + F_F, 1);
      z.fw = ldsf[L_F + x.sw];
      z.fv = ldsf[L_FV + 8 * x.sw + (x.lane & 7)];
      asm volatile("" : "+v"(z.fw), "+v"(z.fv));
    }
    SB();
    slots<N + 1, END>(z, x);
  }
}

__global__ void __launch_bounds__(NTHR, 3)
fused_decode(const char* pk, const char* px, char* pws, const char* pwq, const char* pw1, const char* pw2, const char* pv, const char* pwo, char* pout) {
  __shared__ __attribute__((aligned(16))) unsigned lds[L_END];
  LAS unsigned* ldsu = (LAS unsigned*)lds;
  LAS float* ldsf = (LAS float*)lds;
  LAS char* ldsb = (LAS char*)lds;
  const int tid = threadIdx.x, lane = tid & 63;
  const int w = __builtin_amdgcn_readfirstlane(tid >> 6);
  const int b = blockIdx.x;
  const char* pc0;
  unsigned code_bytes;
  asm volatile("s_getpc_b64 %0\n.Lfd_code_begin:\n\ts_mov_b32 %1, .Lfd_code_end-.Lfd_code_begin" : "=s"(pc0), "=s"(code_bytes));
  const unsigned warm0 = ld4<0>(mk_rsrc(pc0, code_bytes), tid * 64);
  const int h = (b & 7) * 4 + (b >> 6), s = (b >> 3) & 7;
  const int r = lane >> 4, c = lane & 15, hi3 = lane >> 3, lo3 = lane & 7;
  unsigned kvoff = (h * SK + s * 1024) * DH * 2, voff = (w - 1) * 1024 + lane * 16;
  asm volatile("" : "+s"(kvoff), "+v"(voff));

  if (w == 0) {
    __builtin_amdgcn_s_setprio(3);
    const rsrc_t rs = mk_rsrc(pws, WS_END);
    const int g0 = 5 * h + (h < 12 ? h : 12), ng = h < 12 ? 6 : 5;
    asm volatile("" ::"s"(pws));
    const unsigned tstart = now();
    __builtin_amdgcn_raw_buffer_store_b32(0u, rs, b < 8 && lane < 16 ? WS_ACC + b * 128 + lane * 4 : OOB, 0, AUX_SC1);
    SB();
    v4u xv = ld16<0>(mk_rsrc(px, D * 2), lane * 16, 0);
    const unsigned epoch = ld4<AUX_SC1>(rs, WS_EPOCH);
    SB();
    v4u wq[16], w1r[8], w2r[6];
    {
      const rsrc_t rq = mk_rsrc(pwq, D * NH * DH * 2), r1 = mk_rsrc(pw1, D * FF * 2), r2 = mk_rsrc(pw2, FF * D * 2);
      const unsigned oq = ((128 * (s >> 1) + 16 * hi3) * (NH * DH) + h * DH + 64 * (s & 1) + 8 * lo3) * 2;
#pragma unroll
      for (int i = 0; i < 16; ++i) wq[i] = ld16<AUX_NT>(rq, oq, i * (NH * DH * 2));
      SB();
      const unsigned o1 = ((64 * s + 8 * hi3) * FF + 8 * (g0 + (lo3 < ng ? lo3 : ng - 1))) * 2;
#pragma unroll
      for (int i = 0; i < 8; ++i) w1r[i] = ld16<AUX_NT>(r1, o1, i * (FF * 2));
      const unsigned o2 = ((8 * g0 + hi3) * D + 64 * s + 8 * lo3) * 2;
#pragma unroll
      for (int j = 0; j < 6; ++j) w2r[j] = ld16<AUX_NT>(r2, o2, (j < ng ? j : ng - 1) * (8 * D * 2));
    }
    if (lane < 16) ldsu[L_FLAG + lane] = L_FLAG + lane == F_TB ? tstart : 0u;
    SB();
    wg_barrier();
    SB();
    PIN(xv);
    float xf[8];
#pragma unroll
    for (int k = 0; k < 4; ++k) { xf[2 * k] = lo_f(xv[k]); xf[2 * k + 1] = hi_f(xv[k]); }
    float sm = 0.f;
#pragma unroll
    for (int k = 0; k < 8; ++k) sm += xf[k];
    const float mean = wave_sum(sm) * (1.0f / D);
    float sq = 0.f;
#pragma unroll
    for (int k = 0; k < 8; ++k) { const float d = xf[k] - mean; sq = fmaf(d, d, sq); }
    const float var = wave_sum(sq) * (1.0f / D);
    const float mb = rbf(mean), den = rbf(sqrtf(rbf(rbf(var) + 1e-5f)));
    v4u xn;
#pragma unroll
    for (int k = 0; k < 4; ++k) xn[k] = pk_bf16(rbf(xf[2 * k] - mb) / den, rbf(xf[2 * k + 1] - mb) / den);
    if (r == (s >> 1)) *(LAS v4u*)(ldsu + L_XS + c * 4) = xn;
    if ((lane >> 1) == (b >> 3)) *(LAS v4u*)(ldsu + L_XR + (lane & 1) * 4) = xv;
    asm volatile("s_waitcnt lgkmcnt(0)" ::: "memory");
    const v4u xa = *(LAS v4u*)(ldsu + L_XS + hi3 * 8), xb = *(LAS v4u*)(ldsu + L_XS + hi3 * 8 + 4), xc = *(LAS v4u*)(ldsu + L_XS + 32 * (s & 1) + hi3 * 4);
    unsigned tag = __builtin_amdgcn_readfirstlane(epoch) * 1664525u + 1013904223u;
    if (tag == 0u) tag = 1013904223u;
    if (lane == 0) ldsu[F_TAG] = tag;
    put_cell(rs, b < 8 && lane < 4 ? WS_T + b * 128 + lane * 16 : OOB, asf(tstart), asf(xcc_id()), tag);
    const unsigned cell = (r * 8 + lo3) * 16;
    {
      float acc[8], hcc[8], y[4];
#pragma unroll
      for (int k = 0; k < 8; ++k) acc[k] = hcc[k] = 0.f;
#pragma unroll
      for (int i = 0; i < 16; i += 2) {
        const unsigned P = i < 8 ? xa[i >> 1] : xb[(i - 8) >> 1];
        row_fma(acc, wq[i], P & 0xffffu, P << 16);
        row_fma(acc, wq[i + 1], P >> 16, P & 0xffff0000u);
      }
#pragma unroll
      for (int k = 0; k < 4; ++k) y[k] = tr32(acc[k], acc[k + 4]);
      const float u0 = tr16(y[0], y[2]), u1 = tr16(y[1], y[3]);
#pragma unroll
      for (int i = 0; i < 8; i += 2) {
        const unsigned P = xc[i >> 1];
        row_fma(hcc, w1r[i], P & 0xffffu, P << 16);
        row_fma(hcc, w1r[i + 1], P >> 16, P & 0xffff0000u);
      }
#pragma unroll
      for (int k = 0; k < 4; ++k) y[k] = tr32(hcc[k], hcc[k + 4]);
      const float z0 = tr16(y[0], y[2]), z1 = tr16(y[1], y[3]);
      put_cell(rs, WS_X1 + (h * NSPLIT + s) * X1_REC + (lane & 8) * 64 + cell, trd<XOR8>(u0, z0, lane & 8), trd<XOR8>(u1, z1, lane & 8), tag);
    }
    const unsigned warm2 = ld4<AUX_NT>(mk_rsrc(pv + kvoff, 1024 * DH * 2), lane * 65536);
    const unsigned warm3 = ld4<AUX_NT>(mk_rsrc(pwo + h * (DH * D * 2), DH * D * 2), lane * 65536);
    unsigned warm1, tbase;
    {
      v4u g[9];
      unsigned off[9];
#pragma unroll
      for (int i = 0; i < 8; ++i) off[i] = WS_X1 + (h * NSPLIT + 2 * (i & 3) + (hi3 & 1)) * X1_REC + (i >> 2) * 512 + cell;
      off[8] = WS_T + lo3 * 128;
#pragma unroll
      for (int i = 0; i < 9; ++i) g[i] = v4u{0u, 0u, 0u, 0u};
      __builtin_amdgcn_s_sleep(X1DELAY);
      get_cells<9, 8>(rs, off, tag, g);
      KEEP(warm0); KEEP(warm2); KEEP(warm3);
      ldsu[L_Q + 4 * c + r] = pk_bf16((asf(g[0][0]) + asf(g[1][0])) + (asf(g[2][0]) + asf(g[3][0])),
                                      (asf(g[0][2]) + asf(g[1][2])) + (asf(g[2][2]) + asf(g[3][2])));
      const float db = sum_lo3((float)(int)(g[8][0] - tstart)) * 0.125f;
      const unsigned onx = (unsigned)__builtin_amdgcn_ballot_w64(g[8][2] == FINX) & 0xffu;
      const bool fin = (onx ? __builtin_ctz(onx) : 0) == (b & 7);
      tbase = __builtin_amdgcn_readfirstlane(tstart + (int)fminf(fmaxf(db, -300.f), 300.f));
      if (lane == 0) { ldsu[F_TB] = tbase; ldsu[F_FIN] = fin; }
      lds_post(ldsu + F_Q);
      warm1 = ld4<0>(mk_rsrc(pout, fin ? D * 2 : 0), (b >> 3) * 32);
      const float h0 = (asf(g[4][0]) + asf(g[5][0])) + (asf(g[6][0]) + asf(g[7][0]));
      const float h1 = (asf(g[4][2]) + asf(g[5][2])) + (asf(g[6][2]) + asf(g[7][2]));
      const float hp = rbf(trd<XOR8>(h0, h1, lane & 8));
      const float hs = lo3 < ng ? rbf(hp / (1.0f + __expf(-hp))) : 0.f;
      float fa[8];
#pragma unroll
      for (int k = 0; k < 8; ++k) fa[k] = 0.f;
#pragma unroll
      for (int j = 0; j < 6; ++j) {
        const unsigned hj = __builtin_amdgcn_ds_bpermute(((lane & 56) | j) * 4, asu(hs));
        row_fma(fa, w2r[j], hj >> 16, hj);
      }
      ldsf[L_FV + 8 * lo3 + hi3] = tr_hi3(fa, lane);
    }
    {
      const unsigned tplan = tbase + ((CKA * PD) >> 4);
      wait_until(tplan + PA + CKS);
      const unsigned lt = __hip_atomic_load(ldsu + L_LT + lo3, __ATOMIC_RELAXED, __HIP_MEMORY_SCOPE_WORKGROUP);
      const int late = lt ? (int)(lt - tplan) : CKS + 30;
      const unsigned sum = (unsigned)sum_lo3((float)(late < 0 ? 0 : late > 500 ? 500 : late));
      if (lane == 0) __hip_atomic_fetch_add((GAS unsigned*)(pws + WS_ACC + (b & 7) * 128), (1u << 24) + sum, __ATOMIC_RELAXED, __HIP_MEMORY_SCOPE_AGENT);
      wait_until(tplan + PA + CKS + CKR);
      const unsigned a = ld4<AUX_SC1>(rs, WS_ACC + lo3 * 128);
      const float mean = sum_lo3((float)(a & 0xffffffu)) / fmaxf(8.f * sum_lo3((float)(a >> 24)), 1.f);
      if (lane == 0) ldsu[F_TB] = tbase + (int)fminf(fmaxf(mean - CKM, 0.f), 300.f);
    }
    lds_wait(ldsu + F_ML, NSW);
    {
      const v2f ml = *(LAS v2f*)(ldsf + L_ML + 2 * lo3);
      const float M = max_lo3(ml[0]);
      const float L = sum_lo3(ml[1] * __builtin_amdgcn_exp2f((ml[0] - M) * LOG2E));
      put_cell(rs, lane < 4 ? WS_X2 + (h * NSPLIT + s) * X2_REC + lane * 16 : OOB, M, L, tag);
      lds_wait(ldsu + F_PROG, 1);
      v4u g[1] = {{0u, 0u, 0u, 0u}};
      const unsigned off[1] = {WS_X2 + (h * NSPLIT + lo3) * X2_REC};
      get_cells<1, 8>(rs, off, tag, g);
      KEEP(warm1);
      const float Mh = max_lo3(asf(g[0][0]));
      const float Lh = sum_lo3(asf(g[0][2]) * __builtin_amdgcn_exp2f((asf(g[0][0]) - Mh) * LOG2E));
      const float f = __builtin_amdgcn_exp2f((ml[0] - Mh) * LOG2E) / Lh;
      if (lane < 8) ldsf[L_F + lane] = f;
      lds_post(ldsu + F_F);
    }
    asm volatile(".Lfd_code_end:");
    return;
  }

  const int sw = w - 1;
  Ctx x;
  x.rk = mk_rsrc(pk + kvoff, 1024 * DH * 2);
  x.voff = voff;
  Strm z;
#pragma unroll
  for (int i = 0; i < WIN0; ++i) z.buf[i] = ld16<AUX_NT>(x.rk, x.voff, i * 8192);
  SB();
  const rsrc_t rs = mk_rsrc(pws, WS_END);
  x.rs = rs;
  x.rv = mk_rsrc(pv + kvoff, 1024 * DH * 2);
  x.ro = mk_rsrc(pwo, NH * DH * D * 2);
  x.oo = ((h * DH + 16 * sw + 2 * hi3) * D + 64 * s + 8 * lo3) * 2;
  x.toff = PA + ((sw * PD) >> 7);
  x.ldsu = ldsu; x.lane = lane; x.c = c; x.sw = sw; x.h = h; x.s = s; x.idx = b >> 3;
  z.l0 = z.l1 = z.l2 = z.l3 = z.sc[0] = z.sc[1] = 0.f;
#pragma unroll
  for (int k = 0; k < 4; ++k) z.acc[k] = v2f{0.f, 0.f};
  wg_barrier();
  slots<WIN0, 8>(z, x);
  KEEP(warm0);
  slots<8, N_END>(z, x);
  const unsigned tag = z.tag;
  {
    const float y0 = tr32(z.acc[0][0], z.acc[2][0]), y1 = tr32(z.acc[0][1], z.acc[2][1]), y2 = tr32(z.acc[1][0], z.acc[3][0]), y3 = tr32(z.acc[1][1], z.acc[3][1]);
    const v2f o = {tr16(y0, y2) * z.fw, tr16(y1, y3) * z.fw};
    *(LAS v2f*)(ldsb + z.a_red_w) = o;
  }
  wg_barrier();
  {
    const v2f o = *(const LAS v2f*)(ldsb + z.a_red_r);
    float o0 = o[0], o1 = o[1];
    sum_hi3_2(o0, o1);
    put_cell(rs, z.o_h1_w, o0, o1, tag);
    SB();
    float ta[8];
#pragma unroll
    for (int k = 0; k < 8; ++k) { ta[k] = 0.f; PIN(ta[k]); }
    __builtin_amdgcn_s_sleep(D1);
    wait_until(z.tb_end + C1);
    asm volatile("" ::: "memory");
    const v4u t1 = ld16<AUX_SC1>(rs, z.o_h1_r, 0);
    SB();
    unsigned wp[8];
#pragma unroll
    for (int k = 0; k < 4; ++k) {
      wp[2 * k] = __builtin_amdgcn_perm(z.wor[1][k], z.wor[0][k], 0x05040100u);
      wp[2 * k + 1] = __builtin_amdgcn_perm(z.wor[1][k], z.wor[0][k], 0x07060302u);
    }
#pragma unroll
    for (int k = 0; k < 8; ++k) wp[k] = __builtin_amdgcn_ds_bpermute((lo3 * 8 + hi3) * 4, wp[k]);
#pragma unroll
    for (int k = 0; k < 8; ++k) PIN(wp[k]);
    SB();
    const bool hit = cell_ok(t1, tag);
    v4u g[1] = {{hit ? t1[0] : asu(o0), 0u, hit ? t1[2] : asu(o1), 0u}};
    const unsigned off[1] = {hit ? OOB : z.o_h1_r};
    if (__builtin_expect(any_lane(off[0] != OOB), 0)) get_cells<1, 2>(rs, off, tag, g);
    float a0 = asf(g[0][0]), a1 = asf(g[0][2]);
    sum_hi3_2(a0, a1);
    const unsigned ap = pk_bf16(a0, a1);
#pragma unroll
    for (int k = 0; k < 8; ++k) ta[k] = dot2(wp[k], ap, ta[k]);
    *(LAS float*)(ldsb + z.a_tp_w) = tr_lo3(ta, lane);
    wg_barrier();
    put_cell(rs, z.o_h2_w, sum_hi3(*(const LAS float*)(ldsb + z.a_tp_r)), z.fv, tag);
  }
  if (z.fin) {
    v4u g[4];
    unsigned off[4];
#pragma unroll
    for (int i = 0; i < 4; ++i) { g[i] = v4u{0u, 0u, 0u, 0u}; off[i] = z.o_h2_r + i * 8 * H2_REC; }
    const rsrc_t rout = mk_rsrc(pout, D * 2);
    unsigned ooff = lane < 8 ? (16 * (b >> 3) + 8 * sw + lane) * 2 : OOB;
    PIN(ooff);
    __builtin_amdgcn_s_sleep(D2);
    get_cells<4, 2>(rs, off, tag, g);
    float st = (asf(g[0][0]) + asf(g[1][0])) + (asf(g[2][0]) + asf(g[3][0]));
    float sf = (asf(g[0][2]) + asf(g[1][2])) + (asf(g[2][2]) + asf(g[3][2]));
    sum_hi3_2(st, sf);
    const float o = z.xr + rbf(rbf(st) * rbf(silu_f(rbf(sf))));
    __builtin_amdgcn_raw_buffer_store_b16((unsigned short)(pk_bf16(o, o) & 0xffffu), rout, ooff, 0, 0);
    if (b < 8 && sw == 0) put_cell(rs, lane < 8 ? WS_EPOCH + lane * 16 : OOB, asf(tag), asf(tag), tag);
  }
}

}

extern "C" void kernel_launch(void* const* d_in, const int* in_sizes, int n_in, void* d_out, int out_size, void* d_ws,
                              size_t ws_size, hipStream_t stream) {
  (void)in_sizes; (void)n_in; (void)out_size; (void)ws_size;
  fused_decode<<<dim3(NH * NSPLIT), dim3(NTHR), 0, stream>>>(
      (const char*)d_in[1], (const char*)d_in[0], (char*)d_ws, (const char*)d_in[3], (const char*)d_in[6], (const char*)d_in[7],
      (const char*)d_in[2], (const char*)d_in[4], (char*)d_out);
}
```

```cpp
#include <hip/hip_runtime.h>
#include <stdint.h>

#define LAS __attribute__((address_space(3)))
typedef unsigned v4u __attribute__((ext_vector_type(4)));
typedef float v2f __attribute__((ext_vector_type(2)));
typedef __bf16 bf16x2 __attribute__((ext_vector_type(2)));

namespace {

constexpr int WIN = 6, WIN0 = 3, PSTART = 10;
constexpr int PA = 58, PD = 455;
constexpr int QS = 26, DPS = 5;
constexpr int X2SLOT = 50;
constexpr int X1DELAY = 32, C1 = 120, D1 = 4, D2 = 6;
constexpr unsigned FINX = 0;

constexpr int D = 512, NH = 32, SK = 8192, DH = 128, FF = 1376;
constexpr int NSPLIT = 8, NSW = 8, NTHR = 64 * (NSW + 1);
constexpr int NL = SK / NSPLIT / NSW / 4;

constexpr unsigned WS_EPOCH = 0;
constexpr unsigned WS_T = 1024;
constexpr unsigned WS_X1 = 4096, X1_REC = 1024;
constexpr unsigned WS_X2 = WS_X1 + 256 * X1_REC, X2_REC = 128;
constexpr unsigned WS_H1 = WS_X2 + 256 * X2_REC, H1_REC = 1024;
constexpr unsigned WS_H2 = WS_H1 + 256 * H1_REC, H2_REC = 1024;
constexpr unsigned WS_END = WS_H2 + 256 * H2_REC;
constexpr unsigned SPIN_CAP = 1u << 16, OOB = 0x7fffffffu;

constexpr int L_RED = 0;
constexpr int L_TP = L_RED + NSW * 128;
constexpr int L_FV = L_TP + NSW * 64;
constexpr int L_XS = L_FV + 64;
constexpr int L_XR = L_XS + 64;
constexpr int L_Q = L_XR + 8;
constexpr int L_ML = L_Q + 64;
constexpr int L_F = L_ML + 16;
constexpr int L_FLAG = L_F + 8;
constexpr int F_Q = L_FLAG, F_ML = L_FLAG + 1, F_F = L_FLAG + 2, F_TAG = L_FLAG + 3, F_PROG = L_FLAG + 4, F_TB = L_FLAG + 5, F_FIN = L_FLAG + 6;
constexpr int L_END = L_FLAG + 8;

__device__ __forceinline__ float asf(unsigned u) { return __builtin_bit_cast(float, u); }
__device__ __forceinline__ unsigned asu(float f) { return __builtin_bit_cast(unsigned, f); }
__device__ __forceinline__ float lo_f(unsigned u) { return asf(u << 16); }
__device__ __forceinline__ float hi_f(unsigned u) { return asf(u & 0xffff0000u); }
__device__ __forceinline__ unsigned pk_bf16(float lo, float hi) {
  bf16x2 v = {(__bf16)lo, (__bf16)hi};
  return __builtin_bit_cast(unsigned, v);
}
__device__ __forceinline__ float rbf(float x) { return (float)(__bf16)x; }
__device__ __forceinline__ float dot2(unsigned a, unsigned b, float c) {
  return __builtin_amdgcn_fdot2_f32_bf16(__builtin_bit_cast(bf16x2, a), __builtin_bit_cast(bf16x2, b), c, false);
}
template <int CTRL> __device__ __forceinline__ float dpp(float x) {
  return asf(__builtin_amdgcn_mov_dpp(__builtin_bit_cast(int, x), CTRL, 0xf, 0xf, true));
}
constexpr int XOR1 = 0xB1, XOR2 = 0x4E, XOR7 = 0x141, XOR8 = 0x128, NEWBC = 0x150;
__device__ __forceinline__ float tr16(float u, float v) {
  auto s = __builtin_amdgcn_permlane16_swap(asu(u), asu(v), false, false);
  return asf(s[0]) + asf(s[1]);
}
__device__ __forceinline__ float tr32(float u, float v) {
  auto s = __builtin_amdgcn_permlane32_swap(asu(u), asu(v), false, false);
  return asf(s[0]) + asf(s[1]);
}
__device__ __forceinline__ float mx16(float u) {
  auto s = __builtin_amdgcn_permlane16_swap(asu(u), asu(u), false, false);
  return fmaxf(asf(s[0]), asf(s[1]));
}
__device__ __forceinline__ float mx32(float u) {
  auto s = __builtin_amdgcn_permlane32_swap(asu(u), asu(u), false, false);
  return fmaxf(asf(s[0]), asf(s[1]));
}
template <int CTRL> __device__ __forceinline__ float trd(float lo, float hi, bool bit) {
  const float a = lo + dpp<CTRL>(lo), b = hi + dpp<CTRL>(hi);
  return bit ? b : a;
}
__device__ __forceinline__ float sum_lo3(float v) { v += dpp<XOR1>(v); v += dpp<XOR2>(v); v += dpp<XOR7>(v); return v; }
__device__ __forceinline__ float max_lo3(float v) { v = fmaxf(v, dpp<XOR1>(v)); v = fmaxf(v, dpp<XOR2>(v)); v = fmaxf(v, dpp<XOR7>(v)); return v; }
__device__ __forceinline__ float sum_hi3(float v) { v += dpp<XOR8>(v); v = tr16(v, v); v = tr32(v, v); return v; }
__device__ __forceinline__ void sum_hi3_2(float& a, float& b) {
  float t = tr32(a, b);
  t += dpp<XOR8>(t);
  t = tr16(t, t);
  auto s = __builtin_amdgcn_permlane32_swap(asu(t), asu(t), false, false);
  a = asf(s[0]); b = asf(s[1]);
}
__device__ __forceinline__ float wave_sum(float v) { return sum_hi3(sum_lo3(v)); }
__device__ __forceinline__ float wave_max(float v) { v = max_lo3(v); v = fmaxf(v, dpp<XOR8>(v)); return mx32(mx16(v)); }
constexpr float LOG2E = 1.4426950408889634f;
__device__ __forceinline__ float silu_f(float v) { return v * __builtin_amdgcn_rcpf(1.0f + __builtin_amdgcn_exp2f(-LOG2E * v)); }
__device__ __forceinline__ void wg_barrier() {
  asm volatile("s_waitcnt lgkmcnt(0)" ::: "memory");
  __builtin_amdgcn_s_barrier();
  asm volatile("" ::: "memory");
}
#define SB() __builtin_amdgcn_sched_barrier(0)
#define VMWAIT(n) asm volatile("s_waitcnt vmcnt(%0)" ::"i"(n) : "memory")
#define PIN(x) asm volatile("" : "+v"(x))
#define KEEP(x) asm volatile("" ::"v"(x))
constexpr int AUX_NT = 2, AUX_SC1 = 16;
typedef __amdgpu_buffer_rsrc_t rsrc_t;
__device__ __forceinline__ rsrc_t mk_rsrc(const char* p, unsigned bytes) { return __builtin_amdgcn_make_buffer_rsrc((void*)p, 0, (int)bytes, 0x00020000); }
template <int AUX> __device__ __forceinline__ v4u ld16(rsrc_t r, unsigned voff, unsigned soff) { return __builtin_amdgcn_raw_buffer_load_b128(r, voff, soff, AUX); }
template <int AUX> __device__ __forceinline__ unsigned ld4(rsrc_t r, unsigned voff) { return __builtin_amdgcn_raw_buffer_load_b32(r, voff, 0, AUX); }
__device__ __forceinline__ void put_cell(rsrc_t r, unsigned off, float a, float b, unsigned tag) {
  const v4u g = {asu(a), tag, asu(b), tag};
  __builtin_amdgcn_raw_buffer_store_b128(g, r, off, 0, AUX_SC1);
}
__device__ __forceinline__ bool any_lane(bool p) { return __builtin_amdgcn_ballot_w64(p) != 0; }
__device__ __forceinline__ bool cell_ok(const v4u& t, unsigned tag) { return ((t[1] ^ tag) | (t[3] ^ tag)) == 0u; }
template <int N, int SLP> __device__ __forceinline__ void get_cells(rsrc_t r, const unsigned (&off)[N], unsigned tag, v4u (&g)[N]) {
  unsigned o[N];
#pragma unroll
  for (int i = 0; i < N; ++i) o[i] = off[i];
#pragma nounroll
  for (unsigned spins = 0;; ++spins) {
    asm volatile("" ::: "memory");
    v4u t[N];
#pragma unroll
    for (int i = 0; i < N; ++i) t[i] = ld16<AUX_SC1>(r, o[i], 0);
    unsigned pend = OOB;
#pragma unroll
    for (int i = 0; i < N; ++i) {
      const bool hit = cell_ok(t[i], tag);
      g[i][0] = hit ? t[i][0] : g[i][0];
      g[i][2] = hit ? t[i][2] : g[i][2];
      o[i] = hit ? OOB : o[i];
      pend &= o[i];
    }
    if (__builtin_expect(!any_lane(pend != OOB), 1)) break;
    if (spins > SPIN_CAP) {
#pragma unroll
      for (int i = 0; i < N; ++i) g[i][0] = g[i][2] = 0x7fc00000u;
      break;
    }
    __builtin_amdgcn_s_sleep(SLP);
  }
}
__device__ __forceinline__ unsigned lds_get(LAS unsigned* p) {
  return __builtin_amdgcn_readfirstlane(__hip_atomic_load(p, __ATOMIC_RELAXED, __HIP_MEMORY_SCOPE_WORKGROUP));
}
__device__ __forceinline__ void lds_wait(LAS unsigned* p, unsigned want) {
#pragma nounroll
  for (unsigned spins = 0; lds_get(p) < want && spins < (SPIN_CAP << 6); ++spins) __builtin_amdgcn_s_sleep(1);
  asm volatile("" ::: "memory");
}
__device__ __forceinline__ void lds_post(LAS unsigned* p) {
  asm volatile("s_waitcnt lgkmcnt(0)" ::: "memory");
  if ((threadIdx.x & 63) == 0) __hip_atomic_fetch_add(p, 1u, __ATOMIC_RELAXED, __HIP_MEMORY_SCOPE_WORKGROUP);
}
__device__ __forceinline__ unsigned now() { return (unsigned)__builtin_amdgcn_s_memrealtime(); }
__device__ __forceinline__ void wait_until(unsigned t) {
#pragma nounroll
  while ((int)(now() - t) < 0) __builtin_amdgcn_s_sleep(1);
}
__device__ __forceinline__ unsigned xcc_id() { return (unsigned)__builtin_amdgcn_s_getreg(20 | (3 << 11)) & 0xfu; }
__device__ __forceinline__ void row_fma(float (&acc)[8], v4u w, unsigned xlo, unsigned xhi) {
#pragma unroll
  for (int k = 0; k < 4; ++k) {
    acc[2 * k] = dot2(w[k], xlo, acc[2 * k]);
    acc[2 * k + 1] = dot2(w[k], xhi, acc[2 * k + 1]);
  }
}
__device__ __forceinline__ float tr_lo3(const float (&a)[8], int lane) {
  float y[4], z[2];
#pragma unroll
  for (int k = 0; k < 4; ++k) y[k] = trd<XOR7>(a[k], a[k + 4], lane & 4);
#pragma unroll
  for (int k = 0; k < 2; ++k) z[k] = trd<XOR2>(y[k], y[k + 2], lane & 2);
  return trd<XOR1>(z[0], z[1], lane & 1);
}
__device__ __forceinline__ float tr_hi3(const float (&a)[8], int lane) {
  float y[4], z[2];
#pragma unroll
  for (int k = 0; k < 4; ++k) y[k] = tr32(a[k], a[k + 4]);
#pragma unroll
  for (int k = 0; k < 2; ++k) z[k] = tr16(y[k], y[k + 2]);
  return trd<XOR8>(z[0], z[1], lane & 8);
}
constexpr int brev4(int i) { return ((i & 1) << 3) | ((i & 2) << 1) | ((i & 4) >> 1) | ((i & 8) >> 3); }

struct Strm {
  v4u buf[2 * NL], wor[2], q;
  float l0, l1, l2, l3, sc[2], p[2], m, fw, fv, xr;
  v2f acc[4];
  unsigned tag, tb_end;
  unsigned a_red_w, a_red_r, a_tp_w, a_tp_r, o_h1_w, o_h1_r, o_h2_w, o_h2_r;
  bool fin;
};
struct Ctx { rsrc_t rk, rv, ro; unsigned voff, oo, toff; LAS unsigned* ldsu; int lane, c, sw, h, s, idx; };

template <int I> __device__ __forceinline__ void k_dot(Strm& z, int c) {
  const v4u k = z.buf[I];
  float d = dot2(k[0], z.q[0], 0.f);
  d = dot2(k[1], z.q[1], d);
  d = dot2(k[2], z.q[2], d);
  d = dot2(k[3], z.q[3], d);
  if constexpr ((I & 1) == 0) { z.l0 = d; return; }
  d = trd<XOR8>(z.l0, d, c & 8);
  if constexpr ((I & 2) == 0) { z.l1 = d; return; }
  d = trd<XOR7>(z.l1, d, c & 4);
  if constexpr ((I & 4) == 0) { z.l2 = d; return; }
  d = trd<XOR2>(z.l2, d, c & 2);
  if constexpr ((I & 8) == 0) { z.l3 = d; return; }
  z.sc[I >> 4] = trd<XOR1>(z.l3, d, c & 1);
}
template <int I> __device__ __forceinline__ void pv_step(Strm& z) {
  const v4u v = z.buf[NL + I];
  const float pb = dpp<NEWBC + brev4(I & 15)>(z.p[I >> 4]);
  const v2f pp = {pb, pb};
#pragma unroll
  for (int k = 0; k < 4; ++k) { const v2f vf = {lo_f(v[k]), hi_f(v[k])}; z.acc[k] = __builtin_elementwise_fma(pp, vf, z.acc[k]); }
  asm volatile("" : "+v"(z.acc[0]), "+v"(z.acc[1]), "+v"(z.acc[2]), "+v"(z.acc[3]));
}
constexpr int cmin(int a, int b) { return a < b ? a : b; }
constexpr int N_SM = NL - 1 + WIN, N_END = 2 * NL + 4;
constexpr int dots_done(int n) { return n < QS ? 0 : cmin(cmin(n - WIN + 1, NL), (n - QS + 1) * DPS); }
constexpr int pv_done(int n) { return n < N_SM + 2 ? 0 : n >= 2 * NL - 1 ? n - NL - 3 : cmin(n - NL - WIN + 1, 2 * (n - N_SM - 1)); }
static_assert(dots_done(N_SM) == NL && pv_done(N_END - 1) == NL && pv_done(2 * NL - 1) >= pv_done(2 * NL - 2), "slot plan");
static_assert(N_SM + 6 < 2 * NL && N_SM + 1 < X2SLOT && X2SLOT < 2 * NL - 1, "slot plan");
template <int LO, int HI> __device__ __forceinline__ void k_dots(Strm& z, int c) {
  if constexpr (LO < HI) { k_dot<LO>(z, c); k_dots<LO + 1, HI>(z, c); }
}
template <int LO, int HI> __device__ __forceinline__ void pv_steps(Strm& z) {
  if constexpr (LO < HI) { pv_step<LO>(z); pv_steps<LO + 1, HI>(z); }
}
template <int N, int END> __device__ __forceinline__ void slots(Strm& z, const Ctx& x) {
  if constexpr (N < END) {
    LAS float* ldsf = (LAS float*)x.ldsu;
    if constexpr (N < 2 * NL) {
      VMWAIT((N < PSTART ? WIN0 : WIN) - 1);
      if constexpr (N >= PSTART && N <= QS) wait_until(lds_get(x.ldsu + F_TB) + x.toff + ((N * PD) >> 4));
      if constexpr (N > QS) wait_until(z.tb_end + x.toff - (PA + 4 * PD - ((N * PD) >> 4)));
      z.buf[N] = ld16<AUX_NT>(N < NL ? x.rk : x.rv, x.voff, (N % NL) * 8192);
      SB();
    }
    if constexpr (N == QS) {
      lds_wait(x.ldsu + F_Q, 1);
      z.q = *(LAS v4u*)(x.ldsu + L_Q + 4 * x.c);
      z.tag = lds_get(x.ldsu + F_TAG);
      z.tb_end = lds_get(x.ldsu + F_TB) + PA + 4 * PD;
    }
    k_dots<dots_done(N - 1), dots_done(N)>(z, x.c);
    if constexpr (dots_done(N) > dots_done(N - 1)) asm volatile("" : "+v"(z.l0), "+v"(z.l1), "+v"(z.l2), "+v"(z.l3), "+v"(z.sc[0]), "+v"(z.sc[1]));
    if constexpr (N == N_SM) {
      constexpr float SCALE = 0.08838834764831845f;
      const float s0 = rbf(rbf(z.sc[0]) * SCALE), s1 = rbf(rbf(z.sc[1]) * SCALE);
      z.m = wave_max(fmaxf(s0, s1));
      z.p[0] = __builtin_amdgcn_exp2f((s0 - z.m) * LOG2E);
      z.p[1] = __builtin_amdgcn_exp2f((s1 - z.m) * LOG2E);
      asm volatile("" : "+v"(z.p[0]), "+v"(z.p[1]), "+v"(z.m));
    }
    if constexpr (N == N_SM + 1) {
      const v2f ml = {z.m, wave_sum(z.p[0] + z.p[1])};
      if (x.lane == 0) *(LAS v2f*)(ldsf + L_ML + 2 * x.sw) = ml;
      lds_post(x.ldsu + F_ML);
    }
    if constexpr (N == N_SM + 6) {
      const int lane = x.lane, hi3 = lane >> 3, lo3 = lane & 7, sw = x.sw;
      z.a_red_w = (L_RED + sw * 128 + 8 * x.c + 2 * (lane >> 4)) * 4;
      z.a_red_r = (L_RED + hi3 * 128 + 16 * sw + 2 * lo3) * 4;
      z.a_tp_w = (L_TP + sw * 64 + lane) * 4;
      z.a_tp_r = (L_TP + hi3 * 64 + 8 * sw + lo3) * 4;
      z.o_h1_w = lane < 8 ? WS_H1 + (x.h * NSPLIT + x.s) * H1_REC + sw * 128 + lane * 16 : OOB;
      z.o_h1_r = hi3 != x.s ? WS_H1 + (x.h * NSPLIT + hi3) * H1_REC + sw * 128 + lo3 * 16 : OOB;
      z.o_h2_w = lane < 8 ? WS_H2 + (x.s * NH + x.h) * H2_REC + sw * 128 + lane * 16 : OOB;
      const int job = 2 * x.idx + (sw & 1);
      z.o_h2_r = WS_H2 + ((job >> 3) * NH + hi3) * H2_REC + (job & 7) * 128 + lo3 * 16;
      z.xr = lo_f(((LAS unsigned short*)(x.ldsu + L_XR))[8 * (sw & 1) + lo3]);
      z.fin = sw < 2 && lds_get(x.ldsu + F_FIN) != 0u;
      asm volatile("" : "+v"(z.a_red_w), "+v"(z.a_red_r), "+v"(z.a_tp_w), "+v"(z.a_tp_r), "+v"(z.o_h1_w), "+v"(z.o_h1_r), "+v"(z.o_h2_w), "+v"(z.o_h2_r), "+v"(z.xr));
    }
    if constexpr (N == N_END - 1) {
      VMWAIT(0);
      z.wor[0] = ld16<AUX_NT>(x.ro, x.oo, 0);
      z.wor[1] = ld16<AUX_NT>(x.ro, x.oo, D * 2);
      SB();
    }
    pv_steps<pv_done(N - 1), pv_done(N)>(z);
    if constexpr (N == X2SLOT) { if (x.sw == 7) lds_post(x.ldsu + F_PROG); }
    if constexpr (N == 2 * NL - 1) {
      lds_wait(x.ldsu + F_F, 1);
      z.fw = ldsf[L_F + x.sw];
      z.fv = ldsf[L_FV + 8 * x.sw + (x.lane & 7)];
      asm volatile("" : "+v"(z.fw), "+v"(z.fv));
    }
    SB();
    slots<N + 1, END>(z, x);
  }
}

__global__ void __launch_bounds__(NTHR, 3)
fused_decode(const char* pk, const char* px, char* pws, const char* pwq, const char* pw1, const char* pw2, const char* pv, const char* pwo, char* pout) {
  __shared__ __attribute__((aligned(16))) unsigned lds[L_END];
  LAS unsigned* ldsu = (LAS unsigned*)lds;
  LAS float* ldsf = (LAS float*)lds;
  LAS char* ldsb = (LAS char*)lds;
  const int tid = threadIdx.x, lane = tid & 63;
  const int w = __builtin_amdgcn_readfirstlane(tid >> 6);
  const int b = blockIdx.x;
  const char* pc0;
  unsigned code_bytes;
  asm volatile("s_getpc_b64 %0\n.Lfd_code_begin:\n\ts_mov_b32 %1, .Lfd_code_end-.Lfd_code_begin" : "=s"(pc0), "=s"(code_bytes));
  const unsigned warm0 = ld4<0>(mk_rsrc(pc0, code_bytes), tid * 64);
  const int h = (b & 7) * 4 + (b >> 6), s = (b >> 3) & 7;
  const int r = lane >> 4, c = lane & 15, hi3 = lane >> 3, lo3 = lane & 7;
  unsigned kvoff = (h * SK + s * 1024) * DH * 2, voff = (w - 1) * 1024 + lane * 16;
  asm volatile("" : "+s"(kvoff), "+v"(voff));

  if (w == 0) {
    __builtin_amdgcn_s_setprio(3);
    const rsrc_t rs = mk_rsrc(pws, WS_END);
    const int g0 = 5 * h + (h < 12 ? h : 12), ng = h < 12 ? 6 : 5;
    asm volatile("" ::"s"(pws));
    const unsigned tstart = now();
    v4u xv = ld16<0>(mk_rsrc(px, D * 2), lane * 16, 0);
    const unsigned epoch = ld4<AUX_SC1>(rs, WS_EPOCH);
    SB();
    v4u wq[16], w1r[8], w2r[6];
    {
      const rsrc_t rq = mk_rsrc(pwq, D * NH * DH * 2), r1 = mk_rsrc(pw1, D * FF * 2), r2 = mk_rsrc(pw2, FF * D * 2);
      const unsigned oq = ((128 * (s >> 1) + 16 * hi3) * (NH * DH) + h * DH + 64 * (s & 1) + 8 * lo3) * 2;
#pragma unroll
      for (int i = 0; i < 16; ++i) wq[i] = ld16<AUX_NT>(rq, oq, i * (NH * DH * 2));
      SB();
      const unsigned o1 = ((64 * s + 8 * hi3) * FF + 8 * (g0 + (lo3 < ng ? lo3 : ng - 1))) * 2;
#pragma unroll
      for (int i = 0; i < 8; ++i) w1r[i] = ld16<AUX_NT>(r1, o1, i * (FF * 2));
      const unsigned o2 = ((8 * g0 + hi3) * D + 64 * s + 8 * lo3) * 2;
#pragma unroll
      for (int j = 0; j < 6; ++j) w2r[j] = ld16<AUX_NT>(r2, o2, (j < ng ? j : ng - 1) * (8 * D * 2));
    }
    if (lane < 8) ldsu[L_FLAG + lane] = L_FLAG + lane == F_TB ? tstart : 0u;
    SB();
    wg_barrier();
    SB();
    PIN(xv);
    float xf[8];
#pragma unroll
    for (int k = 0; k < 4; ++k) { xf[2 * k] = lo_f(xv[k]); xf[2 * k + 1] = hi_f(xv[k]); }
    float sm = 0.f;
#pragma unroll
    for (int k = 0; k < 8; ++k) sm += xf[k];
    const float mean = wave_sum(sm) * (1.0f / D);
    float sq = 0.f;
#pragma unroll
    for (int k = 0; k < 8; ++k) { const float d = xf[k] - mean; sq = fmaf(d, d, sq); }
    const float var = wave_sum(sq) * (1.0f / D);
    const float mb = rbf(mean), den = rbf(sqrtf(rbf(rbf(var) + 1e-5f)));
    v4u xn;
#pragma unroll
    for (int k = 0; k < 4; ++k) xn[k] = pk_bf16(rbf(xf[2 * k] - mb) / den, rbf(xf[2 * k + 1] - mb) / den);
    if (r == (s >> 1)) *(LAS v4u*)(ldsu + L_XS + c * 4) = xn;
    if ((lane >> 1) == (b >> 3)) *(LAS v4u*)(ldsu + L_XR + (lane & 1) * 4) = xv;
    asm volatile("s_waitcnt lgkmcnt(0)" ::: "memory");
    const v4u xa = *(LAS v4u*)(ldsu + L_XS + hi3 * 8), xb = *(LAS v4u*)(ldsu + L_XS + hi3 * 8 + 4), xc = *(LAS v4u*)(ldsu + L_XS + 32 * (s & 1) + hi3 * 4);
    unsigned tag = __builtin_amdgcn_readfirstlane(epoch) * 1664525u + 1013904223u;
    if (tag == 0u) tag = 1013904223u;
    if (lane == 0) ldsu[F_TAG] = tag;
    put_cell(rs, b < 8 && lane < 4 ? WS_T + b * 128 + lane * 16 : OOB, asf(tstart), asf(xcc_id()), tag);
    const unsigned cell = (r * 8 + lo3) * 16;
    {
      float acc[8], hcc[8], y[4];
#pragma unroll
      for (int k = 0; k < 8; ++k) acc[k] = hcc[k] = 0.f;
#pragma unroll
      for (int i = 0; i < 16; i += 2) {
        const unsigned P = i < 8 ? xa[i >> 1] : xb[(i - 8) >> 1];
        row_fma(acc, wq[i], P & 0xffffu, P << 16);
        row_fma(acc, wq[i + 1], P >> 16, P & 0xffff0000u);
      }
#pragma unroll
      for (int k = 0; k < 4; ++k) y[k] = tr32(acc[k], acc[k + 4]);
      const float u0 = tr16(y[0], y[2]), u1 = tr16(y[1], y[3]);
#pragma unroll
      for (int i = 0; i < 8; i += 2) {
        const unsigned P = xc[i >> 1];
        row_fma(hcc, w1r[i], P & 0xffffu, P << 16);
        row_fma(hcc, w1r[i + 1], P >> 16, P & 0xffff0000u);
      }
#pragma unroll
      for (int k = 0; k < 4; ++k) y[k] = tr32(hcc[k], hcc[k + 4]);
      const float z0 = tr16(y[0], y[2]), z1 = tr16(y[1], y[3]);
      put_cell(rs, WS_X1 + (h * NSPLIT + s) * X1_REC + (lane & 8) * 64 + cell, trd<XOR8>(u0, z0, lane & 8), trd<XOR8>(u1, z1, lane & 8), tag);
    }
    const unsigned warm2 = ld4<AUX_NT>(mk_rsrc(pv + kvoff, 1024 * DH * 2), lane * 65536);
    const unsigned warm3 = ld4<AUX_NT>(mk_rsrc(pwo + h * (DH * D * 2), DH * D * 2), lane * 65536);
    unsigned warm1, warm4;
    {
      v4u g[9];
      unsigned off[9];
#pragma unroll
      for (int i = 0; i < 8; ++i) off[i] = WS_X1 + (h * NSPLIT + 2 * (i & 3) + (hi3 & 1)) * X1_REC + (i >> 2) * 512 + cell;
      off[8] = WS_T + lo3 * 128;
#pragma unroll
      for (int i = 0; i < 9; ++i) g[i] = v4u{0u, 0u, 0u, 0u};
      __builtin_amdgcn_s_sleep(X1DELAY);
      get_cells<9, 8>(rs, off, tag, g);
      KEEP(warm0); KEEP(warm2); KEEP(warm3);
      ldsu[L_Q + 4 * c + r] = pk_bf16((asf(g[0][0]) + asf(g[1][0])) + (asf(g[2][0]) + asf(g[3][0])),
                                      (asf(g[0][2]) + asf(g[1][2])) + (asf(g[2][2]) + asf(g[3][2])));
      const float db = sum_lo3((float)(int)(g[8][0] - tstart)) * 0.125f;
      const unsigned onx = (unsigned)__builtin_amdgcn_ballot_w64(g[8][2] == FINX) & 0xffu;
      const bool fin = (onx ? __builtin_ctz(onx) : 0) == (b & 7);
      if (lane == 0) { ldsu[F_TB] = tstart + (int)fminf(fmaxf(db, -300.f), 300.f); ldsu[F_FIN] = fin; }
      lds_post(ldsu + F_Q);
      warm1 = ld4<0>(mk_rsrc(pout, fin ? D * 2 : 0), (b >> 3) * 32);
      warm4 = ld4<AUX_SC1>(rs, lane < 8 ? WS_H1 + (h * NSPLIT + s) * H1_REC + lane * 128 : lane < 16 ? WS_H2 + (s * NH + h) * H2_REC + lo3 * 128 : OOB);
      const float h0 = (asf(g[4][0]) + asf(g[5][0])) + (asf(g[6][0]) + asf(g[7][0]));
      const float h1 = (asf(g[4][2]) + asf(g[5][2])) + (asf(g[6][2]) + asf(g[7][2]));
      const float hp = rbf(trd<XOR8>(h0, h1, lane & 8));
      const float hs = lo3 < ng ? rbf(hp / (1.0f + __expf(-hp))) : 0.f;
      float fa[8];
#pragma unroll
      for (int k = 0; k < 8; ++k) fa[k] = 0.f;
#pragma unroll
      for (int j = 0; j < 6; ++j) {
        const unsigned hj = __builtin_amdgcn_ds_bpermute(((lane & 56) | j) * 4, asu(hs));
        row_fma(fa, w2r[j], hj >> 16, hj);
      }
      ldsf[L_FV + 8 * lo3 + hi3] = tr_hi3(fa, lane);
    }
    lds_wait(ldsu + F_ML, NSW);
    {
      const v2f ml = *(LAS v2f*)(ldsf + L_ML + 2 * lo3);
      const float M = max_lo3(ml[0]);
      const float L = sum_lo3(ml[1] * __builtin_amdgcn_exp2f((ml[0] - M) * LOG2E));
      put_cell(rs, lane < 4 ? WS_X2 + (h * NSPLIT + s) * X2_REC + lane * 16 : OOB, M, L, tag);
      lds_wait(ldsu + F_PROG, 1);
      v4u g[1] = {{0u, 0u, 0u, 0u}};
      const unsigned off[1] = {WS_X2 + (h * NSPLIT + lo3) * X2_REC};
      get_cells<1, 8>(rs, off, tag, g);
      KEEP(warm1); KEEP(warm4);
      const float Mh = max_lo3(asf(g[0][0]));
      const float Lh = sum_lo3(asf(g[0][2]) * __builtin_amdgcn_exp2f((asf(g[0][0]) - Mh) * LOG2E));
      const float f = __builtin_amdgcn_exp2f((ml[0] - Mh) * LOG2E) / Lh;
      if (lane < 8) ldsf[L_F + lane] = f;
      lds_post(ldsu + F_F);
    }
    asm volatile(".Lfd_code_end:");
    return;
  }

  const int sw = w - 1;
  Ctx x;
  x.rk = mk_rsrc(pk + kvoff, 1024 * DH * 2);
  x.voff = voff;
  Strm z;
#pragma unroll
  for (int i = 0; i < WIN0; ++i) z.buf[i] = ld16<AUX_NT>(x.rk, x.voff, i * 8192);
  SB();
  const rsrc_t rs = mk_rsrc(pws, WS_END);
  x.rv = mk_rsrc(pv + kvoff, 1024 * DH * 2);
  x.ro = mk_rsrc(pwo, NH * DH * D * 2);
  x.oo = ((h * DH + 16 * sw + 2 * hi3) * D + 64 * s + 8 * lo3) * 2;
  x.toff = PA + ((sw * PD) >> 7);
  x.ldsu = ldsu; x.lane = lane; x.c = c; x.sw = sw; x.h = h; x.s = s; x.idx = b >> 3;
  z.l0 = z.l1 = z.l2 = z.l3 = z.sc[0] = z.sc[1] = 0.f;
#pragma unroll
  for (int k = 0; k < 4; ++k) z.acc[k] = v2f{0.f, 0.f};
  wg_barrier();
  slots<WIN0, 8>(z, x);
  KEEP(warm0);
  slots<8, N_END>(z, x);
  const unsigned tag = z.tag;
  {
    const float y0 = tr32(z.acc[0][0], z.acc[2][0]), y1 = tr32(z.acc[0][1], z.acc[2][1]), y2 = tr32(z.acc[1][0], z.acc[3][0]), y3 = tr32(z.acc[1][1], z.acc[3][1]);
    const v2f o = {tr16(y0, y2) * z.fw, tr16(y1, y3) * z.fw};
    *(LAS v2f*)(ldsb + z.a_red_w) = o;
  }
  wg_barrier();
  {
    const v2f o = *(const LAS v2f*)(ldsb + z.a_red_r);
    float o0 = o[0], o1 = o[1];
    sum_hi3_2(o0, o1);
    put_cell(rs, z.o_h1_w, o0, o1, tag);
    SB();
    float ta[8];
#pragma unroll
    for (int k = 0; k < 8; ++k) { ta[k] = 0.f; PIN(ta[k]); }
    __builtin_amdgcn_s_sleep(D1);
    wait_until(z.tb_end + C1);
    asm volatile("" ::: "memory");
    const v4u t1 = ld16<AUX_SC1>(rs, z.o_h1_r, 0);
    SB();
    unsigned wp[8];
#pragma unroll
    for (int k = 0; k < 4; ++k) {
      wp[2 * k] = __builtin_amdgcn_perm(z.wor[1][k], z.wor[0][k], 0x05040100u);
      wp[2 * k + 1] = __builtin_amdgcn_perm(z.wor[1][k], z.wor[0][k], 0x07060302u);
    }
#pragma unroll
    for (int k = 0; k < 8; ++k) wp[k] = __builtin_amdgcn_ds_bpermute((lo3 * 8 + hi3) * 4, wp[k]);
#pragma unroll
    for (int k = 0; k < 8; ++k) PIN(wp[k]);
    SB();
    const bool hit = cell_ok(t1, tag);
    v4u g[1] = {{hit ? t1[0] : asu(o0), 0u, hit ? t1[2] : asu(o1), 0u}};
    const unsigned off[1] = {hit ? OOB : z.o_h1_r};
    if (__builtin_expect(any_lane(off[0] != OOB), 0)) get_cells<1, 2>(rs, off, tag, g);
    float a0 = asf(g[0][0]), a1 = asf(g[0][2]);
    sum_hi3_2(a0, a1);
    const unsigned ap = pk_bf16(a0, a1);
#pragma unroll
    for (int k = 0; k < 8; ++k) ta[k] = dot2(wp[k], ap, ta[k]);
    *(LAS float*)(ldsb + z.a_tp_w) = tr_lo3(ta, lane);
    wg_barrier();
    put_cell(rs, z.o_h2_w, sum_hi3(*(const LAS float*)(ldsb + z.a_tp_r)), z.fv, tag);
  }
  if (z.fin) {
    v4u g[4];
    unsigned off[4];
#pragma unroll
    for (int i = 0; i < 4; ++i) { g[i] = v4u{0u, 0u, 0u, 0u}; off[i] = z.o_h2_r + i * 8 * H2_REC; }
    const rsrc_t rout = mk_rsrc(pout, D * 2);
    unsigned ooff = lane < 8 ? (16 * (b >> 3) + 8 * sw + lane) * 2 : OOB;
    PIN(ooff);
    __builtin_amdgcn_s_sleep(D2);
    get_cells<4, 2>(rs, off, tag, g);
    float st = (asf(g[0][0]) + asf(g[1][0])) + (asf(g[2][0]) + asf(g[3][0]));
    float sf = (asf(g[0][2]) + asf(g[1][2])) + (asf(g[2][2]) + asf(g[3][2]));
    sum_hi3_2(st, sf);
    const float o = z.xr + rbf(rbf(st) * rbf(silu_f(rbf(sf))));
    __builtin_amdgcn_raw_buffer_store_b16((unsigned short)(pk_bf16(o, o) & 0xffffu), rout, ooff, 0, 0);
    if (b < 8 && sw == 0) put_cell(rs, lane < 8 ? WS_EPOCH + lane * 16 : OOB, asf(tag), asf(tag), tag);
  }
}

}

extern "C" void kernel_launch(void* const* d_in, const int* in_sizes, int n_in, void* d_out, int out_size, void* d_ws,
                              size_t ws_size, hipStream_t stream) {
  (void)in_sizes; (void)n_in; (void)out_size; (void)ws_size;
  fused_decode<<<dim3(NH * NSPLIT), dim3(NTHR), 0, stream>>>(
      (const char*)d_in[1], (const char*)d_in[0], (char*)d_ws, (const char*)d_in[3], (const char*)d_in[6], (const char*)d_in[7],
      (const char*)d_in[2], (const char*)d_in[4], (char*)d_out);
}
```

```cpp
#include <hip/hip_runtime.h>
#include <stdint.h>

#define LAS __attribute__((address_space(3)))
typedef unsigned v4u __attribute__((ext_vector_type(4)));
typedef float v2f __attribute__((ext_vector_type(2)));
typedef __bf16 bf16x2 __attribute__((ext_vector_type(2)));

namespace {

constexpr int WIN = 6, WIN0 = 3, PSTART = 10;
constexpr int PA = 54, PD = 455;
constexpr int QS = 26, DPS = 5;
constexpr int X2SLOT = 50;
constexpr int X1DELAY = 40, C1 = 120, D1 = 4, D2 = 7;
constexpr unsigned FINX = 0;

constexpr int D = 512, NH = 32, SK = 8192, DH = 128, FF = 1376;
constexpr int NSPLIT = 8, NSW = 8, NTHR = 64 * (NSW + 1);
constexpr int NL = SK / NSPLIT / NSW / 4;

constexpr unsigned WS_EPOCH = 0;
constexpr unsigned WS_T = 1024;
constexpr unsigned WS_X1 = 4096, X1_REC = 1024;
constexpr unsigned WS_X2 = WS_X1 + 256 * X1_REC, X2_REC = 128;
constexpr unsigned WS_H1 = WS_X1, H1_REC = X1_REC;
constexpr unsigned WS_H2 = WS_X2 + 256 * X2_REC, H2_REC = 1024;
constexpr unsigned WS_END = WS_H2 + 256 * H2_REC;
constexpr unsigned SPIN_CAP = 1u << 18, OOB = 0x7fffffffu;

constexpr int L_RED = 0;
constexpr int L_TP = L_RED + NSW * 128;
constexpr int L_FV = L_TP + NSW * 64;
constexpr int L_XS = L_FV + 64;
constexpr int L_XR = L_XS + 64;
constexpr int L_Q = L_XR + 8;
constexpr int L_ML = L_Q + 64;
constexpr int L_F = L_ML + 16;
constexpr int L_FLAG = L_F + 8;
constexpr int F_Q = L_FLAG, F_ML = L_FLAG + 1, F_F = L_FLAG + 2, F_TAG = L_FLAG + 3, F_PROG = L_FLAG + 4, F_TB = L_FLAG + 5, F_FIN = L_FLAG + 6;
constexpr int L_END = L_FLAG + 8;

__device__ __forceinline__ float asf(unsigned u) { return __builtin_bit_cast(float, u); }
__device__ __forceinline__ unsigned asu(float f) { return __builtin_bit_cast(unsigned, f); }
__device__ __forceinline__ float lo_f(unsigned u) { return asf(u << 16); }
__device__ __forceinline__ float hi_f(unsigned u) { return asf(u & 0xffff0000u); }
__device__ __forceinline__ unsigned pk_bf16(float lo, float hi) {
  bf16x2 v = {(__bf16)lo, (__bf16)hi};
  return __builtin_bit_cast(unsigned, v);
}
__device__ __forceinline__ float rbf(float x) { return (float)(__bf16)x; }
__device__ __forceinline__ float dot2(unsigned a, unsigned b, float c) {
  return __builtin_amdgcn_fdot2_f32_bf16(__builtin_bit_cast(bf16x2, a), __builtin_bit_cast(bf16x2, b), c, false);
}
template <int CTRL> __device__ __forceinline__ float dpp(float x) {
  return asf(__builtin_amdgcn_mov_dpp(__builtin_bit_cast(int, x), CTRL, 0xf, 0xf, true));
}
constexpr int XOR1 = 0xB1, XOR2 = 0x4E, XOR7 = 0x141, XOR8 = 0x128, NEWBC = 0x150;
__device__ __forceinline__ float tr16(float u, float v) {
  auto s = __builtin_amdgcn_permlane16_swap(asu(u), asu(v), false, false);
  return asf(s[0]) + asf(s[1]);
}
__device__ __forceinline__ float tr32(float u, float v) {
  auto s = __builtin_amdgcn_permlane32_swap(asu(u), asu(v), false, false);
  return asf(s[0]) + asf(s[1]);
}
__device__ __forceinline__ float mx16(float u) {
  auto s = __builtin_amdgcn_permlane16_swap(asu(u), asu(u), false, false);
  return fmaxf(asf(s[0]), asf(s[1]));
}
__device__ __forceinline__ float mx32(float u) {
  auto s = __builtin_amdgcn_permlane32_swap(asu(u), asu(u), false, false);
  return fmaxf(asf(s[0]), asf(s[1]));
}
template <int CTRL> __device__ __forceinline__ float trd(float lo, float hi, bool bit) {
  const float a = lo + dpp<CTRL>(lo), b = hi + dpp<CTRL>(hi);
  return bit ? b : a;
}
__device__ __forceinline__ float sum_lo3(float v) { v += dpp<XOR1>(v); v += dpp<XOR2>(v); v += dpp<XOR7>(v); return v; }
__device__ __forceinline__ float max_lo3(float v) { v = fmaxf(v, dpp<XOR1>(v)); v = fmaxf(v, dpp<XOR2>(v)); v = fmaxf(v, dpp<XOR7>(v)); return v; }
__device__ __forceinline__ float sum_hi3(float v) { v += dpp<XOR8>(v); v = tr16(v, v); v = tr32(v, v); return v; }
__device__ __forceinline__ void sum_hi3_2(float& a, float& b) {
  float t = tr32(a, b);
  t += dpp<XOR8>(t);
  t = tr16(t, t);
  auto s = __builtin_amdgcn_permlane32_swap(asu(t), asu(t), false, false);
  a = asf(s[0]); b = asf(s[1]);
}
__device__ __forceinline__ float wave_sum(float v) { return sum_hi3(sum_lo3(v)); }
__device__ __forceinline__ float wave_max(float v) { v = max_lo3(v); v = fmaxf(v, dpp<XOR8>(v)); return mx32(mx16(v)); }
constexpr float LOG2E = 1.4426950408889634f;
__device__ __forceinline__ float silu_f(float v) { return v * __builtin_amdgcn_rcpf(1.0f + __builtin_amdgcn_exp2f(-LOG2E * v)); }
__device__ __forceinline__ void wg_barrier() {
  asm volatile("s_waitcnt lgkmcnt(0)" ::: "memory");
  __builtin_amdgcn_s_barrier();
  asm volatile("" ::: "memory");
}
#define SB() __builtin_amdgcn_sched_barrier(0)
#define VMWAIT(n) asm volatile("s_waitcnt vmcnt(%0)" ::"i"(n) : "memory")
#define PIN(x) asm volatile("" : "+v"(x))
#define KEEP(x) asm volatile("" ::"v"(x))
constexpr int AUX_NT = 2, AUX_SC1 = 16;
typedef __amdgpu_buffer_rsrc_t rsrc_t;
__device__ __forceinline__ rsrc_t mk_rsrc(const char* p, unsigned bytes) { return __builtin_amdgcn_make_buffer_rsrc((void*)p, 0, (int)bytes, 0x00020000); }
template <int AUX> __device__ __forceinline__ v4u ld16(rsrc_t r, unsigned voff, unsigned soff) { return __builtin_amdgcn_raw_buffer_load_b128(r, voff, soff, AUX); }
template <int AUX> __device__ __forceinline__ unsigned ld4(rsrc_t r, unsigned voff) { return __builtin_amdgcn_raw_buffer_load_b32(r, voff, 0, AUX); }
__device__ __forceinline__ void put_cell(rsrc_t r, unsigned off, float a, float b, unsigned tag) {
  const v4u g = {asu(a), tag, asu(b), tag};
  __builtin_amdgcn_raw_buffer_store_b128(g, r, off, 0, AUX_SC1);
}
__device__ __forceinline__ unsigned next_tag(unsigned t) {
  t = t * 1664525u + 1013904223u;
  return t ? t : 1013904223u;
}
__device__ __forceinline__ bool any_lane(bool p) { return __builtin_amdgcn_ballot_w64(p) != 0; }
__device__ __forceinline__ bool cell_ok(const v4u& t, unsigned tag) { return ((t[1] ^ tag) | (t[3] ^ tag)) == 0u; }
template <int N, int SLP> __device__ __forceinline__ void get_cells(rsrc_t r, const unsigned (&off)[N], unsigned tag, v4u (&g)[N]) {
  unsigned o[N];
#pragma unroll
  for (int i = 0; i < N; ++i) o[i] = off[i];
#pragma nounroll
  for (unsigned spins = 0;; ++spins) {
    asm volatile("" ::: "memory");
    v4u t[N];
#pragma unroll
    for (int i = 0; i < N; ++i) t[i] = ld16<AUX_SC1>(r, o[i], 0);
    unsigned pend = OOB;
#pragma unroll
    for (int i = 0; i < N; ++i) {
      const bool hit = cell_ok(t[i], tag);
      g[i][0] = hit ? t[i][0] : g[i][0];
      g[i][2] = hit ? t[i][2] : g[i][2];
      o[i] = hit ? OOB : o[i];
      pend &= o[i];
    }
    if (__builtin_expect(!any_lane(pend != OOB), 1)) break;
    if (spins > SPIN_CAP) {
#pragma unroll
      for (int i = 0; i < N; ++i) g[i][0] = g[i][2] = 0x7fc00000u;
      break;
    }
    __builtin_amdgcn_s_sleep(SLP);
  }
}
__device__ __forceinline__ unsigned lds_get(LAS unsigned* p) {
  return __builtin_amdgcn_readfirstlane(__hip_atomic_load(p, __ATOMIC_RELAXED, __HIP_MEMORY_SCOPE_WORKGROUP));
}
__device__ __forceinline__ void lds_wait(LAS unsigned* p, unsigned want) {
#pragma nounroll
  for (unsigned spins = 0; lds_get(p) < want && spins < (SPIN_CAP << 6); ++spins) __builtin_amdgcn_s_sleep(1);
  asm volatile("" ::: "memory");
}
__device__ __forceinline__ void lds_post(LAS unsigned* p) {
  asm volatile("s_waitcnt lgkmcnt(0)" ::: "memory");
  if ((threadIdx.x & 63) == 0) __hip_atomic_fetch_add(p, 1u, __ATOMIC_RELAXED, __HIP_MEMORY_SCOPE_WORKGROUP);
}
__device__ __forceinline__ unsigned now() { return (unsigned)__builtin_amdgcn_s_memrealtime(); }
__device__ __forceinline__ void wait_until(unsigned t) {
#pragma nounroll
  while ((int)(now() - t) < 0) __builtin_amdgcn_s_sleep(1);
}
__device__ __forceinline__ unsigned xcc_id() { return (unsigned)__builtin_amdgcn_s_getreg(20 | (3 << 11)) & 0xfu; }
__device__ __forceinline__ void row_fma(float (&acc)[8], v4u w, unsigned xlo, unsigned xhi) {
#pragma unroll
  for (int k = 0; k < 4; ++k) {
    acc[2 * k] = dot2(w[k], xlo, acc[2 * k]);
    acc[2 * k + 1] = dot2(w[k], xhi, acc[2 * k + 1]);
  }
}
__device__ __forceinline__ float tr_lo3(const float (&a)[8], int lane) {
  float y[4], z[2];
#pragma unroll
  for (int k = 0; k < 4; ++k) y[k] = trd<XOR7>(a[k], a[k + 4], lane & 4);
#pragma unroll
  for (int k = 0; k < 2; ++k) z[k] = trd<XOR2>(y[k], y[k + 2], lane & 2);
  return trd<XOR1>(z[0], z[1], lane & 1);
}
__device__ __forceinline__ float tr_hi3(const float (&a)[8], int lane) {
  float y[4], z[2];
#pragma unroll
  for (int k = 0; k < 4; ++k) y[k] = tr32(a[k], a[k + 4]);
#pragma unroll
  for (int k = 0; k < 2; ++k) z[k] = tr16(y[k], y[k + 2]);
  return trd<XOR8>(z[0], z[1], lane & 8);
}
constexpr int brev4(int i) { return ((i & 1) << 3) | ((i & 2) << 1) | ((i & 4) >> 1) | ((i & 8) >> 3); }

struct Strm {
  v4u buf[2 * NL], wor[2], q;
  float l0, l1, l2, l3, sc[2], p[2], m, fw, fv, xr;
  v2f acc[4];
  unsigned tag, tb_end;
  unsigned a_red_w, a_red_r, a_tp_w, a_tp_r, o_h1_w, o_h1_r, o_h2_w, o_h2_r;
  bool fin;
};
struct Ctx { rsrc_t rk, rv, ro; unsigned voff, oo, toff; LAS unsigned* ldsu; int lane, c, sw, h, s, idx; };

template <int I> __device__ __forceinline__ void k_dot(Strm& z, int c) {
  const v4u k = z.buf[I];
  float d = dot2(k[0], z.q[0], 0.f);
  d = dot2(k[1], z.q[1], d);
  d = dot2(k[2], z.q[2], d);
  d = dot2(k[3], z.q[3], d);
  if constexpr ((I & 1) == 0) { z.l0 = d; return; }
  d = trd<XOR8>(z.l0, d, c & 8);
  if constexpr ((I & 2) == 0) { z.l1 = d; return; }
  d = trd<XOR7>(z.l1, d, c & 4);
  if constexpr ((I & 4) == 0) { z.l2 = d; return; }
  d = trd<XOR2>(z.l2, d, c & 2);
  if constexpr ((I & 8) == 0) { z.l3 = d; return; }
  z.sc[I >> 4] = trd<XOR1>(z.l3, d, c & 1);
}
template <int I> __device__ __forceinline__ void pv_step(Strm& z) {
  const v4u v = z.buf[NL + I];
  const float pb = dpp<NEWBC + brev4(I & 15)>(z.p[I >> 4]);
  const v2f pp = {pb, pb};
#pragma unroll
  for (int k = 0; k < 4; ++k) { const v2f vf = {lo_f(v[k]), hi_f(v[k])}; z.acc[k] = __builtin_elementwise_fma(pp, vf, z.acc[k]); }
  asm volatile("" : "+v"(z.acc[0]), "+v"(z.acc[1]), "+v"(z.acc[2]), "+v"(z.acc[3]));
}
constexpr int cmin(int a, int b) { return a < b ? a : b; }
constexpr int N_SM = NL - 1 + WIN, N_END = 2 * NL + 4;
constexpr int dots_done(int n) { return n < QS ? 0 : cmin(cmin(n - WIN + 1, NL), (n - QS + 1) * DPS); }
constexpr int pv_done(int n) { return n < N_SM + 2 ? 0 : n >= 2 * NL - 1 ? n - NL - 3 : cmin(n - NL - WIN + 1, 2 * (n - N_SM - 1)); }
static_assert(dots_done(N_SM) == NL && pv_done(N_END - 1) == NL && pv_done(2 * NL - 1) >= pv_done(2 * NL - 2), "slot plan");
static_assert(N_SM + 6 < 2 * NL && N_SM + 1 < X2SLOT && X2SLOT < 2 * NL - 1, "slot plan");
template <int LO, int HI> __device__ __forceinline__ void k_dots(Strm& z, int c) {
  if constexpr (LO < HI) { k_dot<LO>(z, c); k_dots<LO + 1, HI>(z, c); }
}
template <int LO, int HI> __device__ __forceinline__ void pv_steps(Strm& z) {
  if constexpr (LO < HI) { pv_step<LO>(z); pv_steps<LO + 1, HI>(z); }
}
template <int N, int END> __device__ __forceinline__ void slots(Strm& z, const Ctx& x) {
  if constexpr (N < END) {
    LAS float* ldsf = (LAS float*)x.ldsu;
    if constexpr (N < 2 * NL) {
      VMWAIT((N < PSTART ? WIN0 : WIN) - 1);
      if constexpr (N >= PSTART && N <= QS) wait_until(lds_get(x.ldsu + F_TB) + x.toff + ((N * PD) >> 4));
      if constexpr (N > QS) wait_until(z.tb_end + x.toff - (PA + 4 * PD - ((N * PD) >> 4)));
      z.buf[N] = ld16<AUX_NT>(N < NL ? x.rk : x.rv, x.voff, (N % NL) * 8192);
      SB();
    }
    if constexpr (N == QS) {
      lds_wait(x.ldsu + F_Q, 1);
      z.q = *(LAS v4u*)(x.ldsu + L_Q + 4 * x.c);
      z.tag = lds_get(x.ldsu + F_TAG);
      z.tb_end = lds_get(x.ldsu + F_TB) + PA + 4 * PD;
    }
    k_dots<dots_done(N - 1), dots_done(N)>(z, x.c);
    if constexpr (dots_done(N) > dots_done(N - 1)) asm volatile("" : "+v"(z.l0), "+v"(z.l1), "+v"(z.l2), "+v"(z.l3), "+v"(z.sc[0]), "+v"(z.sc[1]));
    if constexpr (N == N_SM) {
      constexpr float SCALE = 0.08838834764831845f;
      const float s0 = rbf(rbf(z.sc[0]) * SCALE), s1 = rbf(rbf(z.sc[1]) * SCALE);
      z.m = wave_max(fmaxf(s0, s1));
      z.p[0] = __builtin_amdgcn_exp2f((s0 - z.m) * LOG2E);
      z.p[1] = __builtin_amdgcn_exp2f((s1 - z.m) * LOG2E);
      asm volatile("" : "+v"(z.p[0]), "+v"(z.p[1]), "+v"(z.m));
    }
    if constexpr (N == N_SM + 1) {
      const v2f ml = {z.m, wave_sum(z.p[0] + z.p[1])};
      if (x.lane == 0) *(LAS v2f*)(ldsf + L_ML + 2 * x.sw) = ml;
      lds_post(x.ldsu + F_ML);
    }
    if constexpr (N == N_SM + 6) {
      const int lane = x.lane, hi3 = lane >> 3, lo3 = lane & 7, sw = x.sw;
      z.a_red_w = (L_RED + sw * 128 + 8 * x.c + 2 * (lane >> 4)) * 4;
      z.a_red_r = (L_RED + hi3 * 128 + 16 * sw + 2 * lo3) * 4;
      z.a_tp_w = (L_TP + sw * 64 + lane) * 4;
      z.a_tp_r = (L_TP + hi3 * 64 + 8 * sw + lo3) * 4;
      z.o_h1_w = lane < 8 ? WS_H1 + (x.h * NSPLIT + x.s) * H1_REC + sw * 128 + lane * 16 : OOB;
      z.o_h1_r = hi3 != x.s ? WS_H1 + (x.h * NSPLIT + hi3) * H1_REC + sw * 128 + lo3 * 16 : OOB;
      z.o_h2_w = lane < 8 ? WS_H2 + (x.s * NH + x.h) * H2_REC + sw * 128 + lane * 16 : OOB;
      const int job = 2 * x.idx + (sw & 1);
      z.o_h2_r = WS_H2 + ((job >> 3) * NH + hi3) * H2_REC + (job & 7) * 128 + lo3 * 16;
      z.xr = lo_f(((LAS unsigned short*)(x.ldsu + L_XR))[8 * (sw & 1) + lo3]);
      z.fin = sw < 2 && lds_get(x.ldsu + F_FIN) != 0u;
      asm volatile("" : "+v"(z.a_red_w), "+v"(z.a_red_r), "+v"(z.a_tp_w), "+v"(z.a_tp_r), "+v"(z.o_h1_w), "+v"(z.o_h1_r), "+v"(z.o_h2_w), "+v"(z.o_h2_r), "+v"(z.xr));
    }
    if constexpr (N == N_END - 1) {
      VMWAIT(0);
      z.wor[0] = ld16<AUX_NT>(x.ro, x.oo, 0);
      z.wor[1] = ld16<AUX_NT>(x.ro, x.oo, D * 2);
      SB();
    }
    pv_steps<pv_done(N - 1), pv_done(N)>(z);
    if constexpr (N == X2SLOT) { if (x.sw == 7) lds_post(x.ldsu + F_PROG); }
    if constexpr (N == 2 * NL - 1) {
      lds_wait(x.ldsu + F_F, 1);
      z.fw = ldsf[L_F + x.sw];
      z.fv = ldsf[L_FV + 8 * x.sw + (x.lane & 7)];
      asm volatile("" : "+v"(z.fw), "+v"(z.fv));
    }
    SB();
    slots<N + 1, END>(z, x);
  }
}

__global__ void __launch_bounds__(NTHR, 3)
fused_decode(const char* pk, const char* px, char* pws, const char* pwq, const char* pw1, const char* pw2, const char* pv, const char* pwo, char* pout) {
  __shared__ __attribute__((aligned(16))) unsigned lds[L_END];
  LAS unsigned* ldsu = (LAS unsigned*)lds;
  LAS float* ldsf = (LAS float*)lds;
  LAS char* ldsb = (LAS char*)lds;
  const int tid = threadIdx.x, lane = tid & 63;
  const int w = __builtin_amdgcn_readfirstlane(tid >> 6);
  const int b = blockIdx.x;
  const char* pc0;
  unsigned code_bytes;
  asm volatile("s_getpc_b64 %0\n.Lfd_code_begin:\n\ts_mov_b32 %1, .Lfd_code_end-.Lfd_code_begin" : "=s"(pc0), "=s"(code_bytes));
  const unsigned warm0 = ld4<0>(mk_rsrc(pc0, code_bytes), tid * 64);
  const int h = (b & 7) * 4 + (b >> 6), s = (b >> 3) & 7;
  const int r = lane >> 4, c = lane & 15, hi3 = lane >> 3, lo3 = lane & 7;
  unsigned kvoff = (h * SK + s * 1024) * DH * 2, voff = (w - 1) * 1024 + lane * 16;
  asm volatile("" : "+s"(kvoff), "+v"(voff));

  if (w == 0) {
    __builtin_amdgcn_s_setprio(3);
    const rsrc_t rs = mk_rsrc(pws, WS_END);
    const int g0 = 5 * h + (h < 12 ? h : 12), ng = h < 12 ? 6 : 5;
    asm volatile("" ::"s"(pws));
    const unsigned tstart = now();
    v4u xv = ld16<0>(mk_rsrc(px, D * 2), lane * 16, 0);
    const unsigned epoch = ld4<AUX_SC1>(rs, WS_EPOCH);
    SB();
    v4u wq[16], w1r[8], w2r[6];
    {
      const rsrc_t rq = mk_rsrc(pwq, D * NH * DH * 2), r1 = mk_rsrc(pw1, D * FF * 2), r2 = mk_rsrc(pw2, FF * D * 2);
      const unsigned oq = ((128 * (s >> 1) + 16 * hi3) * (NH * DH) + h * DH + 64 * (s & 1) + 8 * lo3) * 2;
#pragma unroll
      for (int i = 0; i < 16; ++i) wq[i] = ld16<AUX_NT>(rq, oq, i * (NH * DH * 2));
      SB();
      const unsigned o1 = lo3 < ng ? ((64 * s + 8 * hi3) * FF + 8 * (g0 + lo3)) * 2 : OOB;
#pragma unroll
      for (int i = 0; i < 8; ++i) w1r[i] = ld16<AUX_NT>(r1, o1, i * (FF * 2));
      const unsigned o2 = ((8 * g0 + hi3) * D + 64 * s + 8 * lo3) * 2;
#pragma unroll
      for (int j = 0; j < 6; ++j) w2r[j] = ld16<AUX_NT>(r2, j < ng ? o2 : OOB, j * (8 * D * 2));
    }
    if (lane < 8) ldsu[L_FLAG + lane] = L_FLAG + lane == F_TB ? tstart : 0u;
    SB();
    wg_barrier();
    SB();
    PIN(xv);
    float xf[8];
#pragma unroll
    for (int k = 0; k < 4; ++k) { xf[2 * k] = lo_f(xv[k]); xf[2 * k + 1] = hi_f(xv[k]); }
    float sm = 0.f;
#pragma unroll
    for (int k = 0; k < 8; ++k) sm += xf[k];
    const float mean = wave_sum(sm) * (1.0f / D);
    float sq = 0.f;
#pragma unroll
    for (int k = 0; k < 8; ++k) { const float d = xf[k] - mean; sq = fmaf(d, d, sq); }
    const float var = wave_sum(sq) * (1.0f / D);
    const float mb = rbf(mean), den = rbf(sqrtf(rbf(rbf(var) + 1e-5f)));
    v4u xn;
#pragma unroll
    for (int k = 0; k < 4; ++k) xn[k] = pk_bf16(rbf(xf[2 * k] - mb) / den, rbf(xf[2 * k + 1] - mb) / den);
    if (r == (s >> 1)) *(LAS v4u*)(ldsu + L_XS + c * 4) = xn;
    if ((lane >> 1) == (b >> 3)) *(LAS v4u*)(ldsu + L_XR + (lane & 1) * 4) = xv;
    asm volatile("s_waitcnt lgkmcnt(0)" ::: "memory");
    const v4u xa = *(LAS v4u*)(ldsu + L_XS + hi3 * 8), xb = *(LAS v4u*)(ldsu + L_XS + hi3 * 8 + 4), xc = *(LAS v4u*)(ldsu + L_XS + 32 * (s & 1) + hi3 * 4);
    const unsigned tag = next_tag(__builtin_amdgcn_readfirstlane(epoch));
    if (lane == 0) ldsu[F_TAG] = next_tag(tag);
    put_cell(rs, b < 8 && lane < 4 ? WS_T + b * 128 + lane * 16 : OOB, asf(tstart), asf(xcc_id()), tag);
    const unsigned cell = (r * 8 + lo3) * 16;
    {
      float acc[8], hcc[8], y[4];
#pragma unroll
      for (int k = 0; k < 8; ++k) acc[k] = hcc[k] = 0.f;
#pragma unroll
      for (int i = 0; i < 16; i += 2) {
        const unsigned P = i < 8 ? xa[i >> 1] : xb[(i - 8) >> 1];
        row_fma(acc, wq[i], P & 0xffffu, P << 16);
        row_fma(acc, wq[i + 1], P >> 16, P & 0xffff0000u);
      }
#pragma unroll
      for (int k = 0; k < 4; ++k) y[k] = tr32(acc[k], acc[k + 4]);
      const float u0 = tr16(y[0], y[2]), u1 = tr16(y[1], y[3]);
#pragma unroll
      for (int i = 0; i < 8; i += 2) {
        const unsigned P = xc[i >> 1];
        row_fma(hcc, w1r[i], P & 0xffffu, P << 16);
        row_fma(hcc, w1r[i + 1], P >> 16, P & 0xffff0000u);
      }
#pragma unroll
      for (int k = 0; k < 4; ++k) y[k] = tr32(hcc[k], hcc[k + 4]);
      const float z0 = tr16(y[0], y[2]), z1 = tr16(y[1], y[3]);
      put_cell(rs, WS_X1 + (h * NSPLIT + s) * X1_REC + (lane & 8) * 64 + cell, trd<XOR8>(u0, z0, lane & 8), trd<XOR8>(u1, z1, lane & 8), tag);
    }
    const unsigned warm2 = ld4<AUX_NT>(mk_rsrc(pv + kvoff, 1024 * DH * 2), lane * 65536);
    const unsigned warm3 = ld4<AUX_NT>(mk_rsrc(pwo + h * (DH * D * 2), DH * D * 2), lane * 65536);
    unsigned warm1, warm4;
    {
      v4u g[9];
      unsigned off[9];
#pragma unroll
      for (int i = 0; i < 8; ++i) off[i] = WS_X1 + (h * NSPLIT + 2 * (i & 3) + (hi3 & 1)) * X1_REC + (i >> 2) * 512 + cell;
      off[8] = WS_T + lo3 * 128;
#pragma unroll
      for (int i = 0; i < 9; ++i) g[i] = v4u{0u, 0u, 0u, 0u};
      __builtin_amdgcn_s_sleep(X1DELAY);
      get_cells<9, 8>(rs, off, tag, g);
      KEEP(warm0); KEEP(warm2); KEEP(warm3);
      ldsu[L_Q + 4 * c + r] = pk_bf16((asf(g[0][0]) + asf(g[1][0])) + (asf(g[2][0]) + asf(g[3][0])),
                                      (asf(g[0][2]) + asf(g[1][2])) + (asf(g[2][2]) + asf(g[3][2])));
      const float db = sum_lo3((float)(int)(g[8][0] - tstart)) * 0.125f;
      const unsigned onx = (unsigned)__builtin_amdgcn_ballot_w64(g[8][2] == FINX) & 0xffu;
      const bool fin = (onx ? __builtin_ctz(onx) : 0) == (b & 7);
      if (lane == 0) { ldsu[F_TB] = tstart + (int)fminf(fmaxf(db, -300.f), 300.f); ldsu[F_FIN] = fin; }
      lds_post(ldsu + F_Q);
      warm1 = ld4<0>(mk_rsrc(pout, fin ? D * 2 : 0), (b >> 3) * 32);
      warm4 = ld4<AUX_SC1>(rs, lane < 8 ? WS_H2 + (s * NH + h) * H2_REC + lo3 * 128 : OOB);
      const float h0 = (asf(g[4][0]) + asf(g[5][0])) + (asf(g[6][0]) + asf(g[7][0]));
      const float h1 = (asf(g[4][2]) + asf(g[5][2])) + (asf(g[6][2]) + asf(g[7][2]));
      const float hp = rbf(trd<XOR8>(h0, h1, lane & 8));
      const float hs = lo3 < ng ? rbf(hp / (1.0f + __expf(-hp))) : 0.f;
      float fa[8];
#pragma unroll
      for (int k = 0; k < 8; ++k) fa[k] = 0.f;
#pragma unroll
      for (int j = 0; j < 6; ++j) {
        const unsigned hj = __builtin_amdgcn_ds_bpermute(((lane & 56) | j) * 4, asu(hs));
        row_fma(fa, w2r[j], hj >> 16, hj);
      }
      ldsf[L_FV + 8 * lo3 + hi3] = tr_hi3(fa, lane);
    }
    lds_wait(ldsu + F_ML, NSW);
    {
      const v2f ml = *(LAS v2f*)(ldsf + L_ML + 2 * lo3);
      const float M = max_lo3(ml[0]);
      const float L = sum_lo3(ml[1] * __builtin_amdgcn_exp2f((ml[0] - M) * LOG2E));
      put_cell(rs, lane < 4 ? WS_X2 + (h * NSPLIT + s) * X2_REC + lane * 16 : OOB, M, L, tag);
      lds_wait(ldsu + F_PROG, 1);
      v4u g[1] = {{0u, 0u, 0u, 0u}};
      const unsigned off[1] = {WS_X2 + (h * NSPLIT + lo3) * X2_REC};
      get_cells<1, 8>(rs, off, tag, g);
      KEEP(warm1); KEEP(warm4);
      const float Mh = max_lo3(asf(g[0][0]));
      const float Lh = sum_lo3(asf(g[0][2]) * __builtin_amdgcn_exp2f((asf(g[0][0]) - Mh) * LOG2E));
      const float f = __builtin_amdgcn_exp2f((ml[0] - Mh) * LOG2E) / Lh;
      if (lane < 8) ldsf[L_F + lane] = f;
      lds_post(ldsu + F_F);
    }
    asm volatile(".Lfd_code_end:");
    return;
  }

  const int sw = w - 1;
  Ctx x;
  x.rk = mk_rsrc(pk + kvoff, 1024 * DH * 2);
  x.voff = voff;
  Strm z;
#pragma unroll
  for (int i = 0; i < WIN0; ++i) z.buf[i] = ld16<AUX_NT>(x.rk, x.voff, i * 8192);
  SB();
  const rsrc_t rs = mk_rsrc(pws, WS_END);
  x.rv = mk_rsrc(pv + kvoff, 1024 * DH * 2);
  x.ro = mk_rsrc(pwo, NH * DH * D * 2);
  x.oo = ((h * DH + 16 * sw + 2 * hi3) * D + 64 * s + 8 * lo3) * 2;
  x.toff = PA + ((sw * PD) >> 7);
  x.ldsu = ldsu; x.lane = lane; x.c = c; x.sw = sw; x.h = h; x.s = s; x.idx = b >> 3;
  z.l0 = z.l1 = z.l2 = z.l3 = z.sc[0] = z.sc[1] = 0.f;
#pragma unroll
  for (int k = 0; k < 4; ++k) z.acc[k] = v2f{0.f, 0.f};
  wg_barrier();
  slots<WIN0, 8>(z, x);
  KEEP(warm0);
  slots<8, N_END>(z, x);
  const unsigned tag = z.tag;
  {
    const float y0 = tr32(z.acc[0][0], z.acc[2][0]), y1 = tr32(z.acc[0][1], z.acc[2][1]), y2 = tr32(z.acc[1][0], z.acc[3][0]), y3 = tr32(z.acc[1][1], z.acc[3][1]);
    const v2f o = {tr16(y0, y2) * z.fw, tr16(y1, y3) * z.fw};
    *(LAS v2f*)(ldsb + z.a_red_w) = o;
  }
  wg_barrier();
  {
    const v2f o = *(const LAS v2f*)(ldsb + z.a_red_r);
    float o0 = o[0], o1 = o[1];
    sum_hi3_2(o0, o1);
    put_cell(rs, z.o_h1_w, o0, o1, tag);
    SB();
    float ta[8];
#pragma unroll
    for (int k = 0; k < 8; ++k) { ta[k] = 0.f; PIN(ta[k]); }
    __builtin_amdgcn_s_sleep(D1);
    wait_until(z.tb_end + C1);
    asm volatile("" ::: "memory");
    const v4u t1 = ld16<AUX_SC1>(rs, z.o_h1_r, 0);
    SB();
    unsigned wp[8];
#pragma unroll
    for (int k = 0; k < 4; ++k) {
      wp[2 * k] = __builtin_amdgcn_perm(z.wor[1][k], z.wor[0][k], 0x05040100u);
      wp[2 * k + 1] = __builtin_amdgcn_perm(z.wor[1][k], z.wor[0][k], 0x07060302u);
    }
#pragma unroll
    for (int k = 0; k < 8; ++k) wp[k] = __builtin_amdgcn_ds_bpermute((lo3 * 8 + hi3) * 4, wp[k]);
#pragma unroll
    for (int k = 0; k < 8; ++k) PIN(wp[k]);
    SB();
    const bool hit = cell_ok(t1, tag);
    v4u g[1] = {{hit ? t1[0] : asu(o0), 0u, hit ? t1[2] : asu(o1), 0u}};
    const unsigned off[1] = {hit ? OOB : z.o_h1_r};
    if (__builtin_expect(any_lane(off[0] != OOB), 0)) get_cells<1, 2>(rs, off, tag, g);
    float a0 = asf(g[0][0]), a1 = asf(g[0][2]);
    sum_hi3_2(a0, a1);
    const unsigned ap = pk_bf16(a0, a1);
#pragma unroll
    for (int k = 0; k < 8; ++k) ta[k] = dot2(wp[k], ap, ta[k]);
    *(LAS float*)(ldsb + z.a_tp_w) = tr_lo3(ta, lane);
    wg_barrier();
    put_cell(rs, z.o_h2_w, sum_hi3(*(const LAS float*)(ldsb + z.a_tp_r)), z.fv, tag);
  }
  if (z.fin) {
    v4u g[4];
    unsigned off[4];
#pragma unroll
    for (int i = 0; i < 4; ++i) { g[i] = v4u{0u, 0u, 0u, 0u}; off[i] = z.o_h2_r + i * 8 * H2_REC; }
    const rsrc_t rout = mk_rsrc(pout, D * 2);
    unsigned ooff = lane < 8 ? (16 * (b >> 3) + 8 * sw + lane) * 2 : OOB;
    PIN(ooff);
    __builtin_amdgcn_s_sleep(D2);
    get_cells<4, 2>(rs, off, tag, g);
    float st = (asf(g[0][0]) + asf(g[1][0])) + (asf(g[2][0]) + asf(g[3][0]));
    float sf = (asf(g[0][2]) + asf(g[1][2])) + (asf(g[2][2]) + asf(g[3][2]));
    sum_hi3_2(st, sf);
    const float o = z.xr + rbf(rbf(st) * rbf(silu_f(rbf(sf))));
    __builtin_amdgcn_raw_buffer_store_b16((unsigned short)(pk_bf16(o, o) & 0xffffu), rout, ooff, 0, 0);
    if (b < 8 && sw == 0) put_cell(rs, lane < 8 ? WS_EPOCH + lane * 16 : OOB, asf(tag), asf(tag), tag);
  }
}

}

extern "C" void kernel_launch(void* const* d_in, const int* in_sizes, int n_in, void* d_out, int out_size, void* d_ws,
                              size_t ws_size, hipStream_t stream) {
  (void)in_sizes; (void)n_in; (void)out_size; (void)ws_size;
  fused_decode<<<dim3(NH * NSPLIT), dim3(NTHR), 0, stream>>>(
      (const char*)d_in[1], (const char*)d_in[0], (char*)d_ws, (const char*)d_in[3], (const char*)d_in[6], (const char*)d_in[7],
      (const char*)d_in[2], (const char*)d_in[4], (char*)d_out);
}
```
